```python
import torch

def _softmax(x, axis=-1):
    return torch.softmax(x, dim=axis)

def _gelu(x):
    return torch.nn.functional.gelu(x, approximate='tanh')

def _silu(x):
    return torch.nn.functional.silu(x)

def _sigmoid(x):
    return torch.sigmoid(x)

def _layernorm(x, axis=-1, eps=1e-05):
    m = x.mean(dim=axis, keepdim=True)
    v = x.var(dim=axis, keepdim=True, unbiased=False)
    return (x - m) / torch.sqrt(v + eps)

def _scatter_add(tgt, idx, src, axis=0):
    return tgt.clone().index_add_(axis, idx, src)

def _segment_sum(src, seg, n):
    out = torch.zeros((n,) + src.shape[1:], dtype=src.dtype)
    return out.index_add_(0, seg, src)

def _pad(x, pw):
    flat = tuple((v for pair in reversed(pw) for v in pair))
    return torch.nn.functional.pad(x, flat)
kernel_name = 'adaLN_cond__spectral_fnet__dense_activation_relu__prompt'

def setup_inputs():

    _w = lambda a: (lambda t: t.to(torch.bfloat16) if t.is_floating_point() else t)(torch.as_tensor(a))

    def _A_setup():
        B, S, D = (1, 2048, 512)
        ns = torch.arange(S)[:, None] * torch.arange(S)[None, :]
        nd = torch.arange(D)[:, None] * torch.arange(D)[None, :]
        Ws_re = torch.cos(-2.0 * torch.pi * ns / S).to(float)
        Ws_im = torch.sin(-2.0 * torch.pi * ns / S).to(float)
        Wd_re = torch.cos(-2.0 * torch.pi * nd / D).to(float)
        Wd_im = torch.sin(-2.0 * torch.pi * nd / D).to(float)
        return {'x': _w((lambda *s: torch.randn(*s, dtype=torch.bfloat16) * 0.1)(B, S, D)), 'dft_seq_re': _w(Ws_re), 'dft_seq_im': _w(Ws_im), 'dft_hid_re': _w(Wd_re), 'dft_hid_im': _w(Wd_im)}
    _A_in = _A_setup()

    def _B_setup():
        B, S, D, Dff = (1, 2048, 512, 1376)
        return {'x': _w((lambda *s: torch.randn(*s, dtype=torch.bfloat16) * 0.1)(B, S, D)), 'w1': _w((lambda *s: torch.randn(*s, dtype=torch.bfloat16) * 3.2)(D, Dff)), 'w2': _w((lambda *s: torch.randn(*s, dtype=torch.bfloat16) * 3.2)(Dff, D))}
    _B_in = _B_setup()
    return {'A_x': _A_in['x'], 'A_dft_seq_re': _A_in['dft_seq_re'], 'A_dft_seq_im': _A_in['dft_seq_im'], 'A_dft_hid_re': _A_in['dft_hid_re'], 'A_dft_hid_im': _A_in['dft_hid_im'], 'B_x': _B_in['x'], 'B_w1': _B_in['w1'], 'B_w2': _B_in['w2'], 'c': _w((lambda *s: torch.randn(*s, dtype=torch.bfloat16) * 0.2)(1, 512) * 8.0), 'w_mod': _w((lambda *s: torch.randn(*s, dtype=torch.bfloat16) * 0.2)(512, 6 * 512) * 512 ** (-0.5) * 8.0)}

def reference(A_x, A_dft_seq_re, A_dft_seq_im, A_dft_hid_re, A_dft_hid_im, B_x, B_w1, B_w2, c, w_mod):

    def _A_ref(x, dft_seq_re, dft_seq_im, dft_hid_re, dft_hid_im):
        a = torch.einsum('bsd,ed->bse', x, dft_hid_re)
        b = torch.einsum('bsd,ed->bse', x, dft_hid_im)
        y = torch.einsum('ts,bse->bte', dft_seq_re, a) - torch.einsum('ts,bse->bte', dft_seq_im, b)
        return y

    def _B_ref(x, w1, w2):
        h = torch.einsum('bsd,df->bsf', x, w1)
        zero = torch.zeros_like(h)
        h = torch.maximum(h, zero)
        return torch.einsum('bsf,fd->bsd', h, w2)
    x = A_x
    mod = (_silu(c) @ w_mod).reshape(c.shape[0], 6, 1, -1)
    sa, ca, ga, sb, cb, gb = (mod[:, 0].clone(), mod[:, 1].clone(), mod[:, 2].clone(), mod[:, 3].clone(), mod[:, 4].clone(), mod[:, 5].clone())
    h = _layernorm(x, axis=-1) * (1 + ca) + sa
    x = x + ga * _A_ref(h, A_dft_seq_re, A_dft_seq_im, A_dft_hid_re, A_dft_hid_im)
    h = _layernorm(x, axis=-1) * (1 + cb) + sb
    x = x + gb * _B_ref(h, B_w1, B_w2)
    return x
```

```cpp
#include <hip/hip_runtime.h>
#include <hip/hip_bf16.h>
typedef __hip_bfloat16 bf16;
#define S 2048
#define D 512
#define F 1376
__device__ __forceinline__ float b2f(bf16 v){return __bfloat162float(v);}
__device__ __forceinline__ float rb(float v){return __bfloat162float(__float2bfloat16(v));}

__global__ void k_mod(const bf16* c,const bf16* w,float* mod){
  int j=blockIdx.x*256+threadIdx.x;
  float acc=0;
  for(int k=0;k<D;++k){float x=b2f(c[k]); float s=rb(x/(1.f+expf(-x))); acc+=s*b2f(w[k*3072+j]);}
  mod[j]=rb(acc);
}
__global__ void k_ln(const bf16* x,const float* sc,const float* sh,bf16* h){
  int r=blockIdx.x; int t=threadIdx.x;
  __shared__ float red[256];
  float a=b2f(x[r*D+t]), b=b2f(x[r*D+256+t]);
  red[t]=a+b; __syncthreads();
  for(int o=128;o>0;o>>=1){if(t<o)red[t]+=red[t+o];__syncthreads();}
  float m=red[0]/D; __syncthreads();
  red[t]=(a-m)*(a-m)+(b-m)*(b-m); __syncthreads();
  for(int o=128;o>0;o>>=1){if(t<o)red[t]+=red[t+o];__syncthreads();}
  float v=red[0]/D;
  float rs=rsqrtf(v+1e-5f);
  h[r*D+t]=__float2bfloat16((a-m)*rs*(1.f+sc[t])+sh[t]);
  h[r*D+256+t]=__float2bfloat16((b-m)*rs*(1.f+sc[256+t])+sh[256+t]);
}
template<int MODE>
__global__ void k_gemm(const bf16* A,int lda,const bf16* B,int sbk,int sbn,int K,
                       const bf16* A2,const bf16* B2, bf16* C,int ldc,const bf16* res,const float* g){
  __shared__ float sa[16][17],sb[16][17];
  int tx=threadIdx.x&15,ty=threadIdx.x>>4;
  int m=blockIdx.y*16+ty,n=blockIdx.x*16+tx;
  float acc=0,acc2=0;
  for(int k0=0;k0<K;k0+=16){
    sa[ty][tx]=b2f(A[m*lda+k0+tx]); sb[ty][tx]=b2f(B[(k0+ty)*sbk+n*sbn]); __syncthreads();
    for(int k=0;k<16;++k)acc+=sa[ty][k]*sb[k][tx];
    __syncthreads();
    if(MODE==1){
      sa[ty][tx]=b2f(A2[m*lda+k0+tx]); sb[ty][tx]=b2f(B2[(k0+ty)*sbk+n*sbn]); __syncthreads();
      for(int k=0;k<16;++k)acc2+=sa[ty][k]*sb[k][tx];
      __syncthreads();
    }
  }
  float v=acc;
  if(MODE==1) v=b2f(res[m*ldc+n])+g[n]*(acc-acc2);
  if(MODE==2) v=fmaxf(acc,0.f);
  if(MODE==3) v=b2f(res[m*ldc+n])+g[n]*acc;
  C[m*ldc+n]=__float2bfloat16(v);
}
extern "C" void kernel_launch(void* const* d_in, const int* in_sizes, int n_in,
                              void* d_out, int out_size, void* d_ws, size_t ws_size,
                              hipStream_t stream) {
  const bf16* x=(const bf16*)d_in[0];
  const bf16* wsr=(const bf16*)d_in[1]; const bf16* wsi=(const bf16*)d_in[2];
  const bf16* wdr=(const bf16*)d_in[3]; const bf16* wdi=(const bf16*)d_in[4];
  const bf16* w1=(const bf16*)d_in[6]; const bf16* w2=(const bf16*)d_in[7];
  const bf16* c=(const bf16*)d_in[8]; const bf16* wm=(const bf16*)d_in[9];
  char* ws=(char*)d_ws;
  float* mod=(float*)ws; bf16* h=(bf16*)(ws+(1<<20)); bf16* a=(bf16*)(ws+(4<<20)); bf16* b=(bf16*)(ws+(8<<20));
  bf16* x1=(bf16*)(ws+(12<<20)); bf16* h2=(bf16*)(ws+(16<<20)); bf16* h1=(bf16*)(ws+(20<<20));
  bf16* out=(bf16*)d_out;
  k_mod<<<12,256,0,stream>>>(c,wm,mod);
  k_ln<<<S,256,0,stream>>>(x,mod+512,mod,h);
  k_gemm<0><<<dim3(D/16,S/16),256,0,stream>>>(h,D,wdr,1,D,D,nullptr,nullptr,a,D,nullptr,nullptr);
  k_gemm<0><<<dim3(D/16,S/16),256,0,stream>>>(h,D,wdi,1,D,D,nullptr,nullptr,b,D,nullptr,nullptr);
  k_gemm<1><<<dim3(D/16,S/16),256,0,stream>>>(wsr,S,a,D,1,S,wsi,b,x1,D,x,mod+1024);
  k_ln<<<S,256,0,stream>>>(x1,mod+2048,mod+1536,h2);
  k_gemm<2><<<dim3(F/16,S/16),256,0,stream>>>(h2,D,w1,F,1,D,nullptr,nullptr,h1,F,nullptr,nullptr);
  k_gemm<3><<<dim3(D/16,S/16),256,0,stream>>>(h1,F,w2,D,1,F,nullptr,nullptr,out,D,x1,mod+2560);
}
```

```cpp
#include <hip/hip_runtime.h>
#include <hip/hip_bf16.h>
#include <stdint.h>

typedef unsigned v4u __attribute__((ext_vector_type(4)));
typedef unsigned v2u __attribute__((ext_vector_type(2)));
typedef float f32x4 __attribute__((ext_vector_type(4)));
typedef short bf16x8 __attribute__((ext_vector_type(8)));
typedef __bf16 bf2 __attribute__((ext_vector_type(2)));
typedef float f2 __attribute__((ext_vector_type(2)));
#define GAS __attribute__((address_space(1)))
#define LAS __attribute__((address_space(3)))
#define WAIT_V(n) asm volatile("s_waitcnt vmcnt(%0)" ::"n"(n) : "memory")
#define WAIT_L0() asm volatile("s_waitcnt lgkmcnt(0)" ::: "memory")
#define SCHED() __builtin_amdgcn_sched_barrier(0)

constexpr int S = 2048, D = 512, F = 1376;
constexpr int NKB_PQ = 32, NKB_X = 16, NKB_H = 43;
constexpr size_t OFF_MOD = 0;
constexpr size_t OFF_XNT = 65536;
constexpr size_t OFF_WD  = OFF_XNT + (2u << 20);
constexpr size_t OFF_W1  = OFF_WD + (1u << 20);
constexpr size_t OFF_W2  = OFF_W1 + 86 * 16 * 1024;
constexpr size_t OFF_PQ  = OFF_W2 + 32 * 43 * 1024;
constexpr size_t OFF_X1  = OFF_PQ + (4u << 20);
constexpr size_t OFF_H1  = OFF_X1 + (2u << 20);

__device__ __forceinline__ float bflo(unsigned u) { return __builtin_bit_cast(float, u << 16); }
__device__ __forceinline__ float bfhi(unsigned u) { return __builtin_bit_cast(float, u & 0xffff0000u); }
__device__ __forceinline__ unsigned pk(float lo, float hi) {
  f2 v = {lo, hi};
  bf2 b = __builtin_convertvector(v, bf2);
  return __builtin_bit_cast(unsigned, b);
}
__device__ __forceinline__ float rbf(float x) { return bflo(pk(x, x) & 0xffffu); }
__device__ __forceinline__ f32x4 mfma(v4u a, v4u b, f32x4 c) {
  return __builtin_amdgcn_mfma_f32_16x16x32_bf16(__builtin_bit_cast(bf16x8, a), __builtin_bit_cast(bf16x8, b), c, 0, 0, 0);
}
template <int CTRL> __device__ __forceinline__ float dpp_add(float x) {
  int y = __builtin_amdgcn_update_dpp(0, __builtin_bit_cast(int, x), CTRL, 0xf, 0xf, false);
  return x + __builtin_bit_cast(float, y);
}
__device__ __forceinline__ float wave_sum(float x) {
  x = dpp_add<0x111>(x);
  x = dpp_add<0x112>(x);
  x = dpp_add<0x114>(x);
  x = dpp_add<0x118>(x);
  x = dpp_add<0x142>(x);
  x = dpp_add<0x143>(x);
  return __builtin_bit_cast(float, __builtin_amdgcn_readlane(__builtin_bit_cast(int, x), 63));
}
template <bool NT> __device__ __forceinline__ v4u ldg(const void* p) {
  if (NT) return __builtin_nontemporal_load((const v4u*)p);
  return *(const v4u*)p;
}

#define K1_NT 1

__device__ __forceinline__ void retile8x8(const char* W, int ldw_bytes, int kg, int ng, int nkb, char* dst) {
  v4u R[8];
#pragma unroll
  for (int k = 0; k < 8; ++k) R[k] = ldg<K1_NT>(W + (size_t)(kg * 8 + k) * ldw_bytes + ng * 16);
  const int i = ng >> 2, a = ng & 3, kb = kg >> 2, q = kg & 3;
#pragma unroll
  for (int jn = 0; jn < 8; ++jn) {
    const unsigned sel = (jn & 1) ? 0x07060302u : 0x05040100u;
    const int u = jn >> 1;
    v4u o;
    o.x = __builtin_amdgcn_perm(R[1][u], R[0][u], sel);
    o.y = __builtin_amdgcn_perm(R[3][u], R[2][u], sel);
    o.z = __builtin_amdgcn_perm(R[5][u], R[4][u], sel);
    o.w = __builtin_amdgcn_perm(R[7][u], R[6][u], sel);
    const int tt = jn >> 2, r = jn & 3;
    const int nt = 2 * i + tt, m = 4 * a + r;
    *(v4u*)(dst + ((size_t)(nt * nkb + kb) * 64 + q * 16 + m) * 16) = o;
  }
}

__global__ __launch_bounds__(320) void k1_prep(const char* __restrict__ x, const char* __restrict__ wdr, const char* __restrict__ wdi,
                                               const char* __restrict__ w1, const char* __restrict__ w2, const char* __restrict__ cvec,
                                               const char* __restrict__ wmod, char* __restrict__ ws) {
  const int b = blockIdx.x, lane = threadIdx.x & 63, w = __builtin_amdgcn_readfirstlane(threadIdx.x >> 6);
  if (w == 0) {
    v4u R[8];
#pragma unroll
    for (int r = 0; r < 8; ++r) R[r] = ldg<false>(x + (size_t)(b * 8 + r) * 1024 + lane * 16);
    float v[8][8];
#pragma unroll
    for (int r = 0; r < 8; ++r) {
#pragma unroll
      for (int u = 0; u < 4; ++u) { v[r][2 * u] = bflo(R[r][u]); v[r][2 * u + 1] = bfhi(R[r][u]); }
      float s = 0.f;
#pragma unroll
      for (int j = 0; j < 8; ++j) s += v[r][j];
      const float mean = wave_sum(s) * (1.f / D);
      float ss = 0.f;
#pragma unroll
      for (int j = 0; j < 8; ++j) { const float t = v[r][j] - mean; ss += t * t; }
      const float var = wave_sum(ss) * (1.f / D);
      const float mb = rbf(mean);
      const float den = rbf(__builtin_sqrtf(rbf(rbf(var) + 1e-5f)));
      const float rs = 1.f / den;
#pragma unroll
      for (int j = 0; j < 8; ++j) v[r][j] = rbf(v[r][j] - mb) * rs;
    }
    char* dst = ws + OFF_XNT + (size_t)b * 8192;
    const int i = lane >> 2, a = lane & 3;
#pragma unroll
    for (int j = 0; j < 8; ++j) {
      v4u o;
      o.x = pk(v[0][j], v[1][j]); o.y = pk(v[2][j], v[3][j]); o.z = pk(v[4][j], v[5][j]); o.w = pk(v[6][j], v[7][j]);
      const int oo = 32 * i + 16 * (j >> 2) + 4 * a + (j & 3);
      *(v4u*)(dst + oo * 16) = o;
    }
  } else if (w == 1) {
    if (b < 192) {
      const int chunk = b % 48, kq = b / 48;
      const int r8 = lane >> 3, cc = lane & 7;
      v4u R[16];
#pragma unroll
      for (int i = 0; i < 16; ++i) R[i] = ldg<K1_NT>(wmod + (size_t)(kq * 128 + i * 8 + r8) * 6144 + chunk * 128 + cc * 16);
      float acc[8] = {0.f, 0.f, 0.f, 0.f, 0.f, 0.f, 0.f, 0.f};
#pragma unroll
      for (int i = 0; i < 16; ++i) {
        const float cv = bflo(*(const unsigned short*)(cvec + (kq * 128 + i * 8 + r8) * 2));
        const float sl = rbf(cv / (1.f + __expf(-cv)));
#pragma unroll
        for (int u = 0; u < 4; ++u) { acc[2 * u] += sl * bflo(R[i][u]); acc[2 * u + 1] += sl * bfhi(R[i][u]); }
      }
#pragma unroll
      for (int j = 0; j < 8; ++j) {
        acc[j] += __shfl_xor(acc[j], 8);
        acc[j] += __shfl_xor(acc[j], 16);
        acc[j] += __shfl_xor(acc[j], 32);
      }
      if (lane < 8) {
        float* dst = (float*)(ws + OFF_MOD) + kq * 3072 + chunk * 64 + cc * 8;
        *(f32x4*)dst = f32x4{acc[0], acc[1], acc[2], acc[3]};
        *(f32x4*)(dst + 4) = f32x4{acc[4], acc[5], acc[6], acc[7]};
      }
    }
  } else if (w == 2) {
    if (b < 172) { const int id = b * 64 + lane; retile8x8(w1, F * 2, id / 172, id % 172, NKB_X, ws + OFF_W1); }
  } else if (w == 3) {
    if (b < 172) retile8x8(w2, D * 2, b, lane, NKB_H, ws + OFF_W2);
  } else {
    const int et = b >> 3, m = lane & 15, q = lane >> 4;
    const int e = 32 * (et >> 1) + 8 * (m >> 2) + 4 * (et & 1) + (m & 3);
    const int kb0 = (b & 7) * 4;
    const char* src = (kb0 < 16 ? wdr : wdi) + (size_t)e * 1024 + (kb0 & 15) * 64 + q * 16;
    v4u R[4];
#pragma unroll
    for (int i = 0; i < 4; ++i) R[i] = ldg<false>(src + i * 64);
#pragma unroll
    for (int i = 0; i < 4; ++i) *(v4u*)(ws + OFF_WD + ((size_t)(et * NKB_PQ + kb0 + i) * 64 + lane) * 16) = R[i];
  }
}

__device__ __forceinline__ float mod_at(const char* ws, int idx) {
  const float* m = (const float*)(ws + OFF_MOD) + idx;
  return rbf((m[0] + m[3072]) + (m[6144] + m[9216]));
}

#define K2_NS 5
#define K2_AUXW 0
#define K2_STAG 1
constexpr int K2_STAGE = 24576;
constexpr int K2_LDS = K2_NS * K2_STAGE + 512;

__global__ __launch_bounds__(256, 1) void k2_seq(const char* __restrict__ wsr, const char* __restrict__ wsi, char* __restrict__ ws) {
  extern __shared__ __attribute__((aligned(16))) char shm[];
  constexpr int NS = K2_NS;
  const int b = blockIdx.x, xcd = b & 7, idx = b >> 3, p = xcd * 4 + (idx >> 3), c = idx & 7;
  const int tid = threadIdx.x, lane = tid & 63, w = __builtin_amdgcn_readfirstlane(tid >> 6);
  const int koff = K2_STAG ? ((c * 4 + (idx >> 3) * 8 + xcd) & 31) : 0;
  const char* wbase = (w < 2 ? wsr : wsi) + (size_t)(p * 64 + (w & 1) * 32 + (lane >> 3)) * 4096;
  const int sl = lane & 7, h = lane >> 4;
  const char* wsrc0 = wbase + ((sl ^ h) * 16);
  const char* wsrc1 = wbase + ((sl ^ (4 + h)) * 16);
  const char* xsrc = ws + OFF_XNT + ((size_t)(2 * w) * 512 + c * 64 + lane) * 16;
  auto issue = [&](int kt) {
    const int kk = (kt + koff) & 31;
    LAS char* dst = (LAS char*)shm + (kt % NS) * K2_STAGE;
#pragma unroll
    for (int i = 0; i < 4; ++i)
      __builtin_amdgcn_global_load_lds((const GAS unsigned*)(((i & 1) ? wsrc1 : wsrc0) + (size_t)i * 8 * 4096 + kk * 128),
                                       (LAS unsigned*)(dst + 8192 + (w * 4 + i) * 1024), 16, 0, K2_AUXW);
#pragma unroll
    for (int i = 0; i < 2; ++i)
      __builtin_amdgcn_global_load_lds((const GAS unsigned*)(xsrc + (size_t)kk * 65536 + i * 8192),
                                       (LAS unsigned*)(dst + (2 * w + i) * 1024), 16, 0, 0);
  };
#pragma unroll
  for (int k = 0; k < NS; ++k) issue(k);
  float modv = 0.f;
  if (tid < 128) {
    const int d = c * 64 + (tid & 63);
    modv = tid < 64 ? rbf(1.f + mod_at(ws, 512 + d)) : mod_at(ws, d);
  }
  const int m = lane & 15, q = lane >> 4;
  const int xoff = q * 1024 + m * 16;
  int woff[2][2];
#pragma unroll
  for (int j2 = 0; j2 < 2; ++j2)
#pragma unroll
    for (int kk = 0; kk < 2; ++kk) {
      const int R = 32 * w + 16 * j2 + m;
      woff[j2][kk] = 8192 + R * 128 + (((4 * kk + q) ^ ((R >> 1) & 7)) * 16);
    }
  f32x4 acc[4][2], rs[2];
#pragma unroll
  for (int i = 0; i < 4; ++i) { acc[i][0] = f32x4{0, 0, 0, 0}; acc[i][1] = f32x4{0, 0, 0, 0}; }
  rs[0] = f32x4{0, 0, 0, 0}; rs[1] = f32x4{0, 0, 0, 0};
  const v4u ones = {0x3f803f80u, 0x3f803f80u, 0x3f803f80u, 0x3f803f80u};
  v4u XA[2][4], WA[2][2], XB[2][4], WB[2][2];
  auto rd = [&](int kt, v4u (&X)[2][4], v4u (&W)[2][2]) {
    const char* base = shm + (kt % NS) * K2_STAGE;
#pragma unroll
    for (int kk = 0; kk < 2; ++kk) {
#pragma unroll
      for (int j2 = 0; j2 < 2; ++j2) W[kk][j2] = *(const v4u*)(base + woff[j2][kk]);
#pragma unroll
      for (int i = 0; i < 4; ++i) X[kk][i] = *(const v4u*)(base + xoff + kk * 4096 + i * 256);
    }
  };
  auto mm = [&](v4u (&X)[2][4], v4u (&W)[2][2]) {
#pragma unroll
    for (int kk = 0; kk < 2; ++kk) {
#pragma unroll
      for (int j2 = 0; j2 < 2; ++j2) {
#pragma unroll
        for (int i = 0; i < 4; ++i) acc[i][j2] = mfma(X[kk][i], W[kk][j2], acc[i][j2]);
        rs[j2] = mfma(ones, W[kk][j2], rs[j2]);
      }
    }
  };
  if (tid < 128) ((float*)(shm + NS * K2_STAGE))[tid] = modv;
  WAIT_V(6 * (NS - 1));
  __builtin_amdgcn_s_barrier();
  rd(0, XA, WA);
#define STEP(t, XC, WC, XN, WN, NW, DOISSUE)        \
  {                                                 \
    WAIT_L0();                                      \
    WAIT_V(NW);                                     \
    __builtin_amdgcn_s_barrier();                   \
    SCHED();                                        \
    if (DOISSUE) issue((t) + NS);                   \
    rd((t) + 1, XN, WN);                            \
    mm(XC, WC);                                     \
    SCHED();                                        \
  }
  int t = 0;
  constexpr int NMAIN = ((32 - NS) / 2) * 2;
  for (; t < NMAIN; t += 2) {
    STEP(t, XA, WA, XB, WB, 6 * (NS - 2), true)
    STEP(t + 1, XB, WB, XA, WA, 6 * (NS - 2), true)
  }
#pragma unroll
  for (int tt = NMAIN; tt < 31; ++tt) {
    constexpr int dummy = 0;
    const bool iss = (tt + NS) < 32;
    const int last = (tt - 1 + NS) < 31 ? (tt - 1 + NS) : 31;
    const int allow = 6 * (last - (tt + 1));
    WAIT_L0();
    switch (allow) {
      case 0: WAIT_V(0); break; case 6: WAIT_V(6); break; case 12: WAIT_V(12); break; case 18: WAIT_V(18); break;
      case 24: WAIT_V(24); break; default: WAIT_V(0); break;
    }
    __builtin_amdgcn_s_barrier();
    SCHED();
    if (iss) issue(tt + NS);
    if ((tt - NMAIN) & 1) { rd(tt + 1, XA, WA); mm(XB, WB); } else { rd(tt + 1, XB, WB); mm(XA, WA); }
    SCHED();
  }
  if ((31 - NMAIN) & 1) mm(XB, WB); else mm(XA, WA);
  const float* ml = (const float*)(shm + NS * K2_STAGE);
  const float sgn = (w >> 1) ? -1.f : 1.f;
#pragma unroll
  for (int ip = 0; ip < 2; ++ip) {
    const f32x4 c0 = *(const f32x4*)(ml + 32 * ip + 8 * q), c1 = *(const f32x4*)(ml + 32 * ip + 8 * q + 4);
    const f32x4 s0 = *(const f32x4*)(ml + 64 + 32 * ip + 8 * q), s1 = *(const f32x4*)(ml + 64 + 32 * ip + 8 * q + 4);
#pragma unroll
    for (int j2 = 0; j2 < 2; ++j2) {
      const float r = rs[j2][0];
      const f32x4 a0 = (acc[2 * ip][j2] * c0 + s0 * r) * sgn, a1 = (acc[2 * ip + 1][j2] * c1 + s1 * r) * sgn;
      v4u o; o.x = pk(a0[0], a0[1]); o.y = pk(a0[2], a0[3]); o.z = pk(a1[0], a1[1]); o.w = pk(a1[2], a1[3]);
      const int tt = 4 * p + 2 * (w & 1) + j2, kb = (w >> 1) * 16 + 2 * c + ip;
      *(v4u*)(ws + OFF_PQ + ((size_t)(tt * NKB_PQ + kb) * 64 + lane) * 16) = o;
    }
  }
}

template <int MODE>
__global__ __launch_bounds__(256, 1) void k35(const char* __restrict__ x, char* __restrict__ ws, char* __restrict__ out) {
  __shared__ __attribute__((aligned(16))) char shm[48 * 1024 + 256];
  constexpr int NKB = MODE == 0 ? NKB_PQ : NKB_H;
  constexpr int KW = MODE == 0 ? 8 : 11;
  constexpr int WIN = 3;
  const int b = blockIdx.x, xcd = b & 7, idx = b >> 3, p = xcd * 4 + (idx >> 3), c = idx & 7;
  const int tid = threadIdx.x, lane = tid & 63, w = __builtin_amdgcn_readfirstlane(tid >> 6);
  const char* A = ws + (MODE == 0 ? OFF_WD : OFF_W2) + ((size_t)(4 * c) * NKB + KW * w) * 1024 + lane * 16;
  const char* B = ws + (MODE == 0 ? OFF_PQ : OFF_H1) + ((size_t)(4 * p) * NKB + KW * w) * 1024 + lane * 16;
  const int nk = (MODE == 1 && w == 3) ? 10 : KW;
  v4u fa[WIN][4], fb[WIN][4];
#pragma unroll
  for (int k = 0; k < WIN; ++k)
#pragma unroll
    for (int i = 0; i < 4; ++i) { fa[k][i] = *(const v4u*)(A + (size_t)i * NKB * 1024 + k * 1024); fb[k][i] = *(const v4u*)(B + (size_t)i * NKB * 1024 + k * 1024); }
  const int m = lane & 15, q = lane >> 4;
  v4u res[2];
#pragma unroll
  for (int ip = 0; ip < 2; ++ip) {
    if (MODE == 0) res[ip] = *(const v4u*)(x + (size_t)(p * 64 + w * 16 + m) * 1024 + (c * 64 + ip * 32 + q * 8) * 2);
    else res[ip] = *(const v4u*)(ws + OFF_X1 + ((size_t)((4 * p + w) * NKB_X + 2 * c + ip) * 64 + lane) * 16);
  }
  float gv = 0.f;
  if (tid < 64) gv = mod_at(ws, (MODE == 0 ? 1024 : 2560) + c * 64 + tid);
  SCHED();
  f32x4 acc[4][4];
#pragma unroll
  for (int i = 0; i < 4; ++i)
#pragma unroll
    for (int j = 0; j < 4; ++j) acc[i][j] = f32x4{0, 0, 0, 0};
#pragma unroll
  for (int k = 0; k < KW; ++k) {
    if (k < nk) {
      v4u ca[4], cb[4];
#pragma unroll
      for (int i = 0; i < 4; ++i) { ca[i] = fa[k % WIN][i]; cb[i] = fb[k % WIN][i]; }
      if (k + WIN < KW) {
        const int kn = (k + WIN < nk) ? k + WIN : k;
#pragma unroll
        for (int i = 0; i < 4; ++i) { fa[k % WIN][i] = *(const v4u*)(A + (size_t)i * NKB * 1024 + kn * 1024); fb[k % WIN][i] = *(const v4u*)(B + (size_t)i * NKB * 1024 + kn * 1024); }
      }
#pragma unroll
      for (int i = 0; i < 4; ++i)
#pragma unroll
        for (int j = 0; j < 4; ++j) acc[i][j] = mfma(ca[i], cb[j], acc[i][j]);
    }
    SCHED();
  }
  f32x4* red = (f32x4*)shm;
  float* gl = (float*)(shm + 48 * 1024);
  if (tid < 64) gl[tid] = gv;
#pragma unroll
  for (int j = 0; j < 4; ++j) {
    if (j != w) {
      const int slot = (w < j) ? w : w - 1;
#pragma unroll
      for (int i = 0; i < 4; ++i) red[((j * 3 + slot) * 4 + i) * 64 + lane] = acc[i][j];
    }
  }
  __syncthreads();
  f32x4 y[4];
#pragma unroll
  for (int i = 0; i < 4; ++i) {
    f32x4 t = w == 0 ? acc[i][0] : w == 1 ? acc[i][1] : w == 2 ? acc[i][2] : acc[i][3];
#pragma unroll
    for (int s = 0; s < 3; ++s) t += red[((w * 3 + s) * 4 + i) * 64 + lane];
    y[i] = t;
  }
#pragma unroll
  for (int ip = 0; ip < 2; ++ip) {
    const f32x4 g0 = *(const f32x4*)(gl + 32 * ip + 8 * q), g1 = *(const f32x4*)(gl + 32 * ip + 8 * q + 4);
    const f32x4 a0 = y[2 * ip], a1 = y[2 * ip + 1];
    v4u o;
    o.x = pk(bflo(res[ip].x) + rbf(g0[0] * rbf(a0[0])), bfhi(res[ip].x) + rbf(g0[1] * rbf(a0[1])));
    o.y = pk(bflo(res[ip].y) + rbf(g0[2] * rbf(a0[2])), bfhi(res[ip].y) + rbf(g0[3] * rbf(a0[3])));
    o.z = pk(bflo(res[ip].z) + rbf(g1[0] * rbf(a1[0])), bfhi(res[ip].z) + rbf(g1[1] * rbf(a1[1])));
    o.w = pk(bflo(res[ip].w) + rbf(g1[2] * rbf(a1[2])), bfhi(res[ip].w) + rbf(g1[3] * rbf(a1[3])));
    if (MODE == 0) *(v4u*)(ws + OFF_X1 + ((size_t)((4 * p + w) * NKB_X + 2 * c + ip) * 64 + lane) * 16) = o;
    else *(v4u*)(out + (size_t)(p * 64 + w * 16 + m) * 1024 + (c * 64 + ip * 32 + q * 8) * 2) = o;
  }
}

__global__ __launch_bounds__(256, 1) void k4_ffn1(char* __restrict__ ws) {
  __shared__ __attribute__((aligned(16))) char shm[65536 + 2048];
  constexpr int WIN = 6;
  const int b = blockIdx.x, xcd = b & 7, idx = b >> 3, p = xcd * 4 + (idx >> 3), c = idx & 7;
  const int tid = threadIdx.x, lane = tid & 63, w = __builtin_amdgcn_readfirstlane(tid >> 6);
  const int q = lane >> 4;
  const int ft0 = (c < 6 ? 11 * c : 66 + 10 * (c - 6)), nft = c < 6 ? 11 : 10;
  const int myf0 = ft0 + 3 * w;
  const int myn = (nft - 3 * w) < 3 ? (nft - 3 * w) : 3;
  v4u X[16];
  const char* xs = ws + OFF_X1 + ((size_t)(4 * p + w) * NKB_X * 64 + lane) * 16;
#pragma unroll
  for (int k = 0; k < 16; ++k) X[k] = *(const v4u*)(xs + k * 1024);
  {
    float a0 = rbf(1.f + mod_at(ws, 2048 + 2 * tid)), a1 = rbf(1.f + mod_at(ws, 2048 + 2 * tid + 1));
    float b0 = mod_at(ws, 1536 + 2 * tid), b1 = mod_at(ws, 1536 + 2 * tid + 1);
    ((unsigned*)(shm + 65536))[tid] = pk(a0, a1);
    ((unsigned*)(shm + 65536 + 1024))[tid] = pk(b0, b1);
  }
  const char* A = ws + OFF_W1 + lane * 16;
  int fo[3];
#pragma unroll
  for (int i = 0; i < 3; ++i) fo[i] = (myf0 + (i < myn ? i : 0)) * NKB_X * 1024;
  v4u fa[WIN][3];
#pragma unroll
  for (int k = 0; k < WIN; ++k)
#pragma unroll
    for (int i = 0; i < 3; ++i) fa[k][i] = *(const v4u*)(A + fo[i] + k * 1024);
  SCHED();
  float s = 0.f;
#pragma unroll
  for (int k = 0; k < 16; ++k)
#pragma unroll
    for (int u = 0; u < 4; ++u) s += bflo(X[k][u]) + bfhi(X[k][u]);
  s += __shfl_xor(s, 16); s += __shfl_xor(s, 32);
  const float mean = s * (1.f / D);
  float ss = 0.f;
#pragma unroll
  for (int k = 0; k < 16; ++k)
#pragma unroll
    for (int u = 0; u < 4; ++u) { const float t0 = bflo(X[k][u]) - mean, t1 = bfhi(X[k][u]) - mean; ss += t0 * t0 + t1 * t1; }
  ss += __shfl_xor(ss, 16); ss += __shfl_xor(ss, 32);
  const float mb = rbf(mean);
  const float rsd = 1.f / rbf(__builtin_sqrtf(rbf(rbf(ss * (1.f / D)) + 1e-5f)));
  __syncthreads();
#pragma unroll
  for (int k = 0; k < 16; ++k) {
    const v4u cv = *(const v4u*)(shm + 65536 + k * 64 + q * 16), sv = *(const v4u*)(shm + 65536 + 1024 + k * 64 + q * 16);
    v4u o;
#pragma unroll
    for (int u = 0; u < 4; ++u) {
      const float n0 = rbf(rbf(bflo(X[k][u]) - mb) * rsd), n1 = rbf(rbf(bfhi(X[k][u]) - mb) * rsd);
      o[u] = pk(rbf(n0 * bflo(cv[u])) + bflo(sv[u]), rbf(n1 * bfhi(cv[u])) + bfhi(sv[u]));
    }
    *(v4u*)(shm + (w * 16 + k) * 1024 + lane * 16) = o;
  }
  __syncthreads();
  f32x4 acc[3][4];
#pragma unroll
  for (int i = 0; i < 3; ++i)
#pragma unroll
    for (int j = 0; j < 4; ++j) acc[i][j] = f32x4{0, 0, 0, 0};
#pragma unroll
  for (int k = 0; k < 16; ++k) {
    v4u ca[3], hb[4];
#pragma unroll
    for (int j = 0; j < 4; ++j) hb[j] = *(const v4u*)(shm + (j * 16 + k) * 1024 + lane * 16);
#pragma unroll
    for (int i = 0; i < 3; ++i) ca[i] = fa[k % WIN][i];
    if (k + WIN < 16) {
#pragma unroll
      for (int i = 0; i < 3; ++i) fa[k % WIN][i] = *(const v4u*)(A + fo[i] + (k + WIN) * 1024);
    }
#pragma unroll
    for (int i = 0; i < 3; ++i)
#pragma unroll
      for (int j = 0; j < 4; ++j) acc[i][j] = mfma(ca[i], hb[j], acc[i][j]);
  }
#pragma unroll
  for (int i = 0; i < 3; ++i) {
    if (i < myn) {
      const int ft = myf0 + i;
#pragma unroll
      for (int j = 0; j < 4; ++j) {
        const f32x4 a = acc[i][j];
        v2u o; o.x = pk(fmaxf(a[0], 0.f), fmaxf(a[1], 0.f)); o.y = pk(fmaxf(a[2], 0.f), fmaxf(a[3], 0.f));
        *(v2u*)(ws + OFF_H1 + ((size_t)((4 * p + j) * NKB_H + (ft >> 1)) * 64 + lane) * 16 + (ft & 1) * 8) = o;
      }
    }
  }
}

extern "C" void kernel_launch(void* const* d_in, const int* in_sizes, int n_in, void* d_out, int out_size, void* d_ws, size_t ws_size,
                              hipStream_t stream) {
  const char* x = (const char*)d_in[0];
  const char* wsr = (const char*)d_in[1]; const char* wsi = (const char*)d_in[2];
  const char* wdr = (const char*)d_in[3]; const char* wdi = (const char*)d_in[4];
  const char* w1 = (const char*)d_in[6]; const char* w2 = (const char*)d_in[7];
  const char* c = (const char*)d_in[8]; const char* wm = (const char*)d_in[9];
  char* ws = (char*)d_ws;
  hipFuncSetAttribute((const void*)k2_seq, hipFuncAttributeMaxDynamicSharedMemorySize, K2_LDS);
  k1_prep<<<256, 320, 0, stream>>>(x, wdr, wdi, w1, w2, c, wm, ws);
  k2_seq<<<256, 256, K2_LDS, stream>>>(wsr, wsi, ws);
  k35<0><<<256, 256, 0, stream>>>(x, ws, (char*)d_out);
  k4_ffn1<<<256, 256, 0, stream>>>(ws);
  k35<1><<<256, 256, 0, stream>>>(x, ws, (char*)d_out);
}
```

```cpp
#include <hip/hip_runtime.h>
#include <hip/hip_bf16.h>
#include <stdint.h>

typedef unsigned v4u __attribute__((ext_vector_type(4)));
typedef unsigned v2u __attribute__((ext_vector_type(2)));
typedef float f32x4 __attribute__((ext_vector_type(4)));
typedef float f32x2 __attribute__((ext_vector_type(2)));
typedef short bf16x8 __attribute__((ext_vector_type(8)));
typedef __bf16 bf2 __attribute__((ext_vector_type(2)));
#define GAS __attribute__((address_space(1)))
#define LAS __attribute__((address_space(3)))
typedef GAS unsigned gu32;
#define RLX_AGENT __ATOMIC_RELAXED, __HIP_MEMORY_SCOPE_AGENT
#define WAIT_V(n) asm volatile("s_waitcnt vmcnt(%0)" ::"n"(n) : "memory")
#define WAIT_L0() do { asm volatile("" ::: "memory"); __builtin_amdgcn_s_waitcnt(0xC07F); } while (0)
#define SCHED() __builtin_amdgcn_sched_barrier(0)
#define CFENCE() asm volatile("" ::: "memory")
#define BAR() do { CFENCE(); __builtin_amdgcn_s_barrier(); CFENCE(); } while (0)
#define RSRC(p) __builtin_amdgcn_make_buffer_rsrc((void*)(p), 0, 0x7fffffff, 0x00020000)
#define RSRC2(p) __builtin_amdgcn_make_buffer_rsrc((void*)(p), 0, 2048 * 1024, 0x00020000)
constexpr int SC1 = 16, NTP = 2;

#define STAMP_BEGIN()
#define STAMP_MID(i) {}
#define STAMP_END(k, blk, ws) {}

constexpr int S = 2048, D = 512, F = 1376;
constexpr int NKB_X = 16, NKB_H = 43;
constexpr unsigned OFF_FLAG = 0;
constexpr unsigned OFF_MOD  = 32768;
constexpr unsigned OFF_RS   = 65536;
constexpr unsigned OFF_ST   = 131072;
constexpr unsigned OFF_XNT  = 262144;
constexpr unsigned OFF_WD   = OFF_XNT + (2u << 20);
constexpr unsigned OFF_W1   = OFF_WD + (1u << 20);
constexpr unsigned OFF_W2   = OFF_W1 + 86 * 16 * 1024;
constexpr unsigned OFF_PQ   = OFF_W2 + 32 * 43 * 1024;
constexpr unsigned OFF_X1   = OFF_PQ + (8u << 20);
constexpr unsigned OFF_H1   = OFF_X1 + (2u << 20);

__device__ __forceinline__ float bflo(unsigned u) { return __builtin_bit_cast(float, u << 16); }
__device__ __forceinline__ float bfhi(unsigned u) { return __builtin_bit_cast(float, u & 0xffff0000u); }
__device__ __forceinline__ unsigned pk(float lo, float hi) {
  f32x2 v = {lo, hi};
  return __builtin_bit_cast(unsigned, __builtin_convertvector(v, bf2));
}
__device__ __forceinline__ float rbf(float x) { return bflo(pk(x, x)); }
__device__ __forceinline__ f32x4 mfma(v4u a, v4u b, f32x4 c) {
  return __builtin_amdgcn_mfma_f32_16x16x32_bf16(__builtin_bit_cast(bf16x8, a), __builtin_bit_cast(bf16x8, b), c, 0, 0, 0);
}
template <int CTRL> __device__ __forceinline__ float dpp_add(float x) {
  int y = __builtin_amdgcn_update_dpp(0, __builtin_bit_cast(int, x), CTRL, 0xf, 0xf, false);
  return x + __builtin_bit_cast(float, y);
}
__device__ __forceinline__ float wave_sum(float x) {
  x = dpp_add<0x111>(x); x = dpp_add<0x112>(x); x = dpp_add<0x114>(x); x = dpp_add<0x118>(x);
  x = dpp_add<0x142>(x); x = dpp_add<0x143>(x);
  return __builtin_bit_cast(float, __builtin_amdgcn_readlane(__builtin_bit_cast(int, x), 63));
}
__device__ __forceinline__ float quad16_sum(float x) {
  float y = x;
  asm volatile("s_nop 1\n\tv_permlane16_swap_b32 %0, %1" : "+v"(x), "+v"(y));
  x += y;
  y = x;
  asm volatile("s_nop 1\n\tv_permlane32_swap_b32 %0, %1" : "+v"(x), "+v"(y));
  return x + y;
}
__device__ __forceinline__ v4u ldnt(const void* p) { return __builtin_nontemporal_load((const v4u*)p); }
template <int AUX> __device__ __forceinline__ v4u bld(__amdgpu_buffer_rsrc_t rs, unsigned voff, unsigned soff) { return __builtin_amdgcn_raw_buffer_load_b128(rs, voff, soff, AUX); }
template <int AUX> __device__ __forceinline__ void bst(__amdgpu_buffer_rsrc_t rs, unsigned voff, unsigned soff, v4u v) { __builtin_amdgcn_raw_buffer_store_b128(v, rs, voff + soff, 0, AUX); }
template <int AUX> __device__ __forceinline__ void glds16(const char* g, LAS char* l) {
  __builtin_amdgcn_global_load_lds((const GAS unsigned*)g, (LAS unsigned*)l, 16, 0, AUX);
}

#define KOFFMUL 0
#define K1_PFAUX 2
#define K1_PF 0

__device__ __forceinline__ void retile64(const char* W, int ldw_bytes, int k0, int n0, int kmax, int nmax, int nkb, __amdgpu_buffer_rsrc_t rs,
                                         unsigned dst, char* lds, int lane) {
  const int kg = lane >> 3, ng = lane & 7;
  const bool ok = (k0 + kg * 8 < kmax) && (n0 + ng * 8 < nmax);
  const char* src = W + (size_t)(ok ? k0 + kg * 8 : 0) * ldw_bytes + (ok ? n0 + ng * 8 : 0) * 2;
  v4u R[8];
#pragma unroll
  for (int k = 0; k < 8; ++k) R[k] = ldnt(src + (size_t)k * ldw_bytes);
  const int a = ng & 3, q = kg & 3;
  char* wp = lds + ((ng >> 2) * 4 + (kg >> 2)) * 1024 + (q * 16 + 4 * a) * 16;
#pragma unroll
  for (int jn = 0; jn < 8; ++jn) {
    const unsigned sel = (jn & 1) ? 0x07060302u : 0x05040100u;
    const int u = jn >> 1;
    v4u o;
    o.x = __builtin_amdgcn_perm(R[1][u], R[0][u], sel);
    o.y = __builtin_amdgcn_perm(R[3][u], R[2][u], sel);
    o.z = __builtin_amdgcn_perm(R[5][u], R[4][u], sel);
    o.w = __builtin_amdgcn_perm(R[7][u], R[6][u], sel);
    *(v4u*)(wp + (jn >> 2) * 2048 + (jn & 3) * 16) = o;
  }
  __builtin_amdgcn_fence(__ATOMIC_RELEASE, "wavefront");
  __builtin_amdgcn_wave_barrier();
  __builtin_amdgcn_fence(__ATOMIC_ACQUIRE, "wavefront");
#pragma unroll
  for (int f = 0; f < 8; ++f) {
    const int ntl = f >> 1, kbl = f & 1;
    const v4u o = *(const v4u*)(lds + f * 1024 + lane * 16);
    if (k0 + kbl * 32 < kmax && n0 + (ntl >> 1) * 32 < nmax)
      bst<0>(rs, lane * 16, dst + (((n0 >> 4) + ntl) * nkb + (k0 >> 5) + kbl) * 1024, o);
  }
}

constexpr int K1_NMOD = 96, K1_NJOB = 120, K1_GRID = 256 + K1_NMOD + K1_NJOB;
__global__ __launch_bounds__(512) void k1_prep(const char* __restrict__ x, const char* __restrict__ wsr, const char* __restrict__ wsi,
                                               const char* __restrict__ wdr, const char* __restrict__ wdi,
                                               const char* __restrict__ w1, const char* __restrict__ w2, const char* __restrict__ cvec,
                                               const char* __restrict__ wmod, char* __restrict__ ws) {
  __shared__ __attribute__((aligned(16))) char shm[32768];
  STAMP_BEGIN()
  const int tid = threadIdx.x, lane = tid & 63, w = __builtin_amdgcn_readfirstlane(tid >> 6);
  const __amdgpu_buffer_rsrc_t rs = RSRC(ws);
  if (blockIdx.x < K1_NMOD) {
    const int chunk = blockIdx.x % 48, kh = blockIdx.x / 48, r8 = lane >> 3, cc = lane & 7;
    const int k0 = kh * 256 + w * 32 + r8 * 4;
    v4u R[4];
#pragma unroll
    for (int i = 0; i < 4; ++i) R[i] = ldnt(wmod + (size_t)(k0 + i) * 6144 + chunk * 128 + cc * 16);
    const v2u cv = *(const v2u*)(cvec + k0 * 2);
    float acc[8] = {0.f, 0.f, 0.f, 0.f, 0.f, 0.f, 0.f, 0.f};
#pragma unroll
    for (int i = 0; i < 4; ++i) {
      const float c0 = (i & 1) ? bfhi(cv[i >> 1]) : bflo(cv[i >> 1]);
      const float sl = rbf(c0 / (1.f + __expf(-c0)));
#pragma unroll
      for (int u = 0; u < 4; ++u) { acc[2 * u] += sl * bflo(R[i][u]); acc[2 * u + 1] += sl * bfhi(R[i][u]); }
    }
    float* red = (float*)shm;
    *(f32x4*)(red + (w * 8 + r8) * 68 + cc * 8) = f32x4{acc[0], acc[1], acc[2], acc[3]};
    *(f32x4*)(red + (w * 8 + r8) * 68 + cc * 8 + 4) = f32x4{acc[4], acc[5], acc[6], acc[7]};
    __syncthreads();
    float s = 0.f;
#pragma unroll
    for (int i = 0; i < 8; ++i) s += red[(r8 * 8 + i) * 68 + w * 8 + cc];
    s = dpp_add<0x128>(s);
    s = quad16_sum(s);
    if (lane < 8) ((float*)(ws + OFF_MOD))[kh * 3072 + chunk * 64 + w * 8 + lane] = s;
    if (blockIdx.x < 3 && tid < 32) ((unsigned*)(ws + OFF_FLAG))[(blockIdx.x * 32 + tid) * 64] = 0u;
    if (w == 0) STAMP_END(5, blockIdx.x, ws)
    return;
  }
  if (blockIdx.x < K1_NMOD + K1_NJOB) {
    if (w >= 4) return;
    const int j = (blockIdx.x - K1_NMOD) * 4 + w;
    char* lds = shm + w * 8192;
    if (j < 176) retile64(w1, F * 2, (j & 7) * 64, (j >> 3) * 64, D, F, NKB_X, rs, OFF_W1, lds, lane);
    else if (j < 352) retile64(w2, D * 2, ((j - 176) >> 3) * 64, ((j - 176) & 7) * 64, F, D, NKB_H, rs, OFF_W2, lds, lane);
    else {
      const int jj = j - 352, et = jj >> 2, kb0 = (jj & 3) * 8, m = lane & 15, q = lane >> 4;
      const int e = 32 * (et >> 1) + 8 * (m >> 2) + 4 * (et & 1) + (m & 3);
      const char* src = (kb0 < 16 ? wdr : wdi) + (size_t)e * 1024 + (kb0 & 15) * 64 + q * 16;
      v4u R[8];
#pragma unroll
      for (int i = 0; i < 8; ++i) R[i] = *(const v4u*)(src + i * 64);
#pragma unroll
      for (int i = 0; i < 8; ++i) bst<0>(rs, lane * 16, OFF_WD + (et * 32 + kb0 + i) * 1024, R[i]);
    }
    if (w == 0) STAMP_END(5, blockIdx.x, ws)
    return;
  }
  const int b = blockIdx.x - (K1_NMOD + K1_NJOB);
  const v4u R = ldnt(x + (size_t)(b * 8 + w) * 1024 + lane * 16);
  if (K1_PF > 0) {
    const int xcd = b & 7, idx = b >> 3, pl = idx >> 3, sub = idx & 7;
    const int kh = sub >> 2, gq = sub & 3;
#pragma unroll
    for (int i = 0; i < K1_PF / 2; ++i) {
      const int ins = w * (K1_PF / 2) + i;
      const int row = (xcd * 4 + pl) * 64 + (gq & 1) * 32 + (ins & 3) * 8 + (lane >> 3);
      const int kk = (pl * KOFFMUL + (ins >> 2)) & 15;
      glds16<K1_PFAUX>((gq < 2 ? wsr : wsi) + (size_t)row * 4096 + kh * 2048 + kk * 128 + (lane & 7) * 16, (LAS char*)shm + 8192 + (ins & 15) * 1024);
    }
  }
  float v[8];
#pragma unroll
  for (int u = 0; u < 4; ++u) { v[2 * u] = bflo(R[u]); v[2 * u + 1] = bfhi(R[u]); }
  const float mean = wave_sum(((v[0] + v[1]) + (v[2] + v[3])) + ((v[4] + v[5]) + (v[6] + v[7]))) * (1.f / D);
  float ss = 0.f;
#pragma unroll
  for (int j = 0; j < 8; ++j) { const float t = v[j] - mean; ss += t * t; }
  const float var = wave_sum(ss) * (1.f / D);
  const float mb = rbf(mean);
  const float rsd = 1.f / rbf(__builtin_sqrtf(rbf(rbf(var) + 1e-5f)));
  v4u o;
#pragma unroll
  for (int u = 0; u < 4; ++u) {
    const unsigned d2 = pk(v[2 * u] - mb, v[2 * u + 1] - mb);
    o[u] = pk(bflo(d2) * rsd, bfhi(d2) * rsd);
  }
  *(v4u*)(shm + w * 1024 + lane * 16) = o;
  WAIT_L0();
  BAR();
  const int d = (tid & ~31) + 8 * ((tid >> 2) & 3) + 4 * ((tid >> 4) & 1) + (tid & 3);
  const unsigned short* sp = (const unsigned short*)shm + d;
  v4u t;
#pragma unroll
  for (int u = 0; u < 4; ++u) t[u] = (unsigned)sp[(2 * u) * 512] | ((unsigned)sp[(2 * u + 1) * 512] << 16);
  bst<0>(rs, tid * 16, OFF_XNT + b * 8192, t);
  if (w == 0) STAMP_END(0, b, ws)
}

#define CLUSTER 1
#define AUX_W 2
#define WD_A 1
#define WD_B 2
#define W1_NB1 4
constexpr int NS = 4, STAGE = 32768;
constexpr int L_MOD = NS * STAGE;
constexpr int L_MB = L_MOD + 12288;
constexpr int L_ZERO = L_MB + 6144;
constexpr int L_CNT = L_ZERO + 64;
constexpr int L_RSTD = L_CNT + 64;
constexpr int K2_LDS = L_RSTD + 256;
constexpr int L_RED = 0;
constexpr int L_H2 = 49152;
constexpr unsigned SPIN_CAP = 1u << 16;

__device__ __forceinline__ void signal(char* shm, int which, char* ws, int p, int lane) {
  if (lane == 0) {
    const unsigned old = __hip_atomic_fetch_add((LAS unsigned*)(shm + L_CNT) + which, 1u, __ATOMIC_RELAXED, __HIP_MEMORY_SCOPE_WORKGROUP);
    if (old == 3u) __hip_atomic_fetch_add((gu32*)(ws + OFF_FLAG) + (which * 32 + p) * 64, 1u, RLX_AGENT);
  }
}
__device__ __forceinline__ void wait_flag(char* ws, int which, int p) {
  gu32* f = (gu32*)(ws + OFF_FLAG) + (which * 32 + p) * 64;
#pragma unroll 1
  for (unsigned spins = 0; spins < SPIN_CAP; ++spins) {
    if ((unsigned)__builtin_amdgcn_readfirstlane(__hip_atomic_load(f, RLX_AGENT)) >= 8u) break;
    __builtin_amdgcn_s_sleep(1);
  }
  __builtin_amdgcn_fence(__ATOMIC_ACQUIRE, "wavefront");
  SCHED();
}

__global__ __launch_bounds__(512, 2) void k2_mega(const char* __restrict__ x, const char* __restrict__ wsr, const char* __restrict__ wsi,
                                                  char* __restrict__ ws, char* __restrict__ out) {
  extern __shared__ __attribute__((aligned(16))) char shm[];
  STAMP_BEGIN()
  const int b = blockIdx.x, xcd = b & 7, idx = b >> 3, pl = idx >> 3, p = xcd * 4 + pl, c = idx & 7, dq = c & 3, kh = c >> 2;
  const int tid = threadIdx.x, lane = tid & 63, w8 = __builtin_amdgcn_readfirstlane(tid >> 6), w = w8 & 3;
  const int m = lane & 15, q = lane >> 4;
  const __amdgpu_buffer_rsrc_t rs = RSRC(ws);
  const float* modl = (const float*)(shm + L_MOD);
  const unsigned l16 = lane * 16;
  const int r4 = (2 * c + 4 * pl) & 15;

  if (w8 >= 4) {
    {
      const int gq = (w + dq + 1) & 3;
      const char* wbase = (gq < 2 ? wsr : wsi) + (size_t)(p * 64 + (gq & 1) * 32 + (lane >> 3)) * 4096 + kh * 2048;
      const int sl = lane & 7, h = lane >> 4;
      const char* wsrc0 = wbase + ((sl ^ h) * 16);
      const char* wsrc1 = wbase + ((sl ^ (4 + h)) * 16) + 8 * 4096;
      const char* xsrc = ws + OFF_XNT + ((size_t)(kh * 128 + 2 * w) * 512 + dq * 128 + lane) * 16;
      auto issW = [&](int st) {
        const int kk = (st + pl * KOFFMUL) & 15;
        LAS char* dst = (LAS char*)shm + (st & 3) * STAGE + w * 4096 + 16384;
#pragma unroll
        for (int i = 0; i < 4; ++i) glds16<AUX_W>(((i & 1) ? wsrc1 : wsrc0) + (size_t)(i >> 1) * 16 * 4096 + kk * 128, dst + i * 1024);
      };
      auto issX = [&](int st) {
        const int kk = (st + pl * KOFFMUL) & 15;
        LAS char* dst = (LAS char*)shm + (st & 3) * STAGE + w * 4096;
#pragma unroll
        for (int i = 0; i < 4; ++i) glds16<0>(xsrc + (size_t)kk * 65536 + (i >> 1) * 8192 + (i & 1) * 1024, dst + i * 1024);
      };
      issW(0); issX(0); issW(1); issX(1);
      WAIT_V(8);
      BAR();
      issW(2); issW(3); issX(2); issX(3);
      WAIT_V(16); BAR();
      issX(4);
#pragma unroll 1
      for (int t = 1; t < 11; t += 2) {
        WAIT_V(8); BAR();
        issW(t + 3); issW(t + 4);
        WAIT_V(12); BAR();
        issX(t + 4); issX(t + 5);
      }
      WAIT_V(8); BAR();
      issW(14); issW(15);
#define TAIL12 12
#define TAIL13 4
      SCHED();
      STAMP_MID(0)
      WAIT_V(12); BAR(); SCHED();
      issX(15);
    }
    SCHED();
    const int r3 = (c + 2 * pl) & 7;
    v4u wd[8][4], res[2];
    {
      const unsigned a0 = OFF_WD + ((4 * c) * 32 + 8 * w) * 1024;
      auto ldwd = [&](int k) {
#pragma unroll
        for (int i = 0; i < 4; ++i) wd[k][i] = bld<0>(rs, l16, a0 + (i * 32 + ((k + r3) & 7)) * 1024);
      };
#pragma unroll
      for (int k = 0; k < WD_A; ++k) ldwd(k);
      SCHED();
      WAIT_V(8 + 4 * WD_A); BAR(); SCHED();
#pragma unroll
      for (int k = WD_A; k < WD_A + WD_B; ++k) ldwd(k);
      SCHED();
      WAIT_V(4 * (WD_A + WD_B)); BAR(); SCHED();
#pragma unroll
      for (int ip = 0; ip < 2; ++ip) res[ip] = bld<NTP>(RSRC2(x), m * 1024 + q * 16 + ip * 64, (p * 64 + w * 16) * 1024 + c * 128);
#pragma unroll
      for (int k = WD_A + WD_B; k < 8; ++k) ldwd(k);
    }
    SCHED();
    f32x4 acc[4][4];
    {
      f32x4 accS[4];
#pragma unroll
      for (int i = 0; i < 4; ++i) accS[i] = f32x4{0, 0, 0, 0};
#pragma unroll
      for (int k = 0; k < 8; ++k) {
        const v4u sf = *(const v4u*)(shm + L_MB + ((8 * (w & 1) + ((k + r3) & 7)) * 32 + q * 8) * 2);
#pragma unroll
        for (int i = 0; i < 4; ++i) accS[i] = mfma(wd[k][i], sf, accS[i]);
      }
      SCHED();
      STAMP_MID(1)
      wait_flag(ws, 0, p);
      STAMP_MID(2)
      f32x2 rr[4];
#pragma unroll
      for (int j = 0; j < 4; ++j) rr[j] = __builtin_bit_cast(f32x2, __builtin_amdgcn_raw_buffer_load_b64(rs, m * 32, OFF_RS + (p * 2 + (w >> 1)) * 512 + ((j + w) & 3) * 8, SC1));
      constexpr int WIN = 2;
      v4u pq[WIN][4];
      unsigned b0[4];
#pragma unroll
      for (int j = 0; j < 4; ++j) b0[j] = OFF_PQ + ((4 * p + ((j + w) & 3)) * 64 + 16 * w) * 1024;
#pragma unroll
      for (int g = 0; g < WIN; ++g)
#pragma unroll
        for (int j = 0; j < 4; ++j) pq[g][j] = bld<SC1>(rs, l16, b0[j] + ((g + 2 * r3) & 15) * 1024);
      SCHED();
#pragma unroll
      for (int g = 0; g < 16; ++g) {
#pragma unroll
        for (int j = 0; j < 4; ++j) {
          const v4u cur = pq[g % WIN][j];
#pragma unroll
          for (int i = 0; i < 4; ++i) acc[i][j] = mfma(wd[g >> 1][i], cur, g == 0 ? accS[i] * (rr[j][0] + rr[j][1]) : acc[i][j]);
          if (g + WIN < 16) pq[g % WIN][j] = bld<SC1>(rs, l16, b0[j] + ((g + WIN + 2 * r3) & 15) * 1024);
          SCHED();
        }
      }
    }
    STAMP_MID(3)
    f32x4* red = (f32x4*)(shm + L_RED);
    auto reduce_gate = [&](const float* g) {
#pragma unroll
      for (int j = 1; j < 4; ++j) {
#pragma unroll
        for (int i = 0; i < 4; ++i) red[((((j + w) & 3) * 3 + j - 1) * 4 + i) * 64 + lane] = acc[i][j];
      }
      WAIT_L0();
      BAR();
      SCHED();
#pragma unroll
      for (int i = 0; i < 4; ++i) {
        f32x4 t = acc[i][0];
#pragma unroll
        for (int s = 0; s < 3; ++s) t += red[((w * 3 + s) * 4 + i) * 64 + lane];
        t *= *(const f32x4*)(g + 32 * (i >> 1) + 8 * q + 4 * (i & 1));
        const unsigned r0 = res[i >> 1][2 * (i & 1)], r1 = res[i >> 1][2 * (i & 1) + 1];
        res[i >> 1][2 * (i & 1)] = pk(bflo(r0) + t[0], bfhi(r0) + t[1]);
        res[i >> 1][2 * (i & 1) + 1] = pk(bflo(r1) + t[2], bfhi(r1) + t[3]);
      }
    };
    {
      reduce_gate(modl + 1024 + c * 64);
      float v[16];
#pragma unroll
      for (int u = 0; u < 8; ++u) { v[2 * u] = bflo(res[u >> 2][u & 3]); v[2 * u + 1] = bfhi(res[u >> 2][u & 3]); }
      float s = 0.f;
#pragma unroll
      for (int u = 0; u < 16; u += 4) s += (v[u] + v[u + 1]) + (v[u + 2] + v[u + 3]);
      const float mc = quad16_sum(s) * (1.f / 64);
#pragma unroll
      for (int u = 0; u < 16; ++u) v[u] -= mc;
#pragma unroll
      for (int ip = 0; ip < 2; ++ip) {
        const f32x4 c0 = *(const f32x4*)(modl + 2048 + c * 64 + 32 * ip + 8 * q), c1 = *(const f32x4*)(modl + 2048 + c * 64 + 32 * ip + 8 * q + 4);
        v4u o;
        o.x = pk(v[8 * ip] * c0[0], v[8 * ip + 1] * c0[1]); o.y = pk(v[8 * ip + 2] * c0[2], v[8 * ip + 3] * c0[3]);
        o.z = pk(v[8 * ip + 4] * c1[0], v[8 * ip + 5] * c1[1]); o.w = pk(v[8 * ip + 6] * c1[2], v[8 * ip + 7] * c1[3]);
        bst<SC1>(rs, l16, OFF_X1 + ((4 * p + w) * NKB_X + 2 * c + ip) * 1024, o);
      }
      float ss = 0.f, s2 = 0.f;
#pragma unroll
      for (int u = 0; u < 16; u += 2) { ss += v[u] * v[u]; s2 += v[u + 1] * v[u + 1]; }
      ss = quad16_sum(ss + s2);
      if (q == 0) {
        v2u o = {__builtin_bit_cast(unsigned, mc), __builtin_bit_cast(unsigned, ss)};
        __builtin_amdgcn_raw_buffer_store_b64(o, rs, m * 64, OFF_ST + ((p * 64 + w * 16) * 8 + c) * 8, SC1);
      }
      WAIT_V(0);
      signal(shm, 1, ws, p, lane);
    }
    STAMP_MID(4)
    wait_flag(ws, 1, p);
    STAMP_MID(5)
    {
      v4u X[16];
#pragma unroll
      for (int k = 0; k < 16; ++k) X[k] = bld<SC1>(rs, l16, OFF_X1 + ((4 * p + w) * NKB_X + ((k + r4) & 15)) * 1024);
      f32x4 st[4];
#pragma unroll
      for (int i = 0; i < 4; ++i) st[i] = __builtin_bit_cast(f32x4, bld<SC1>(rs, m * 64 + i * 16, OFF_ST + (p * 64 + w * 16) * 64));
      SCHED();
#pragma unroll
      for (int k = 0; k < 16; ++k) {
        *(v4u*)(shm + L_H2 + (k * 4 + w) * 1024 + l16) = X[k];
        if (k == 3) { WAIT_L0(); BAR(); SCHED(); }
      }
      float mean = 0.f;
#pragma unroll
      for (int i = 0; i < 4; ++i) mean += st[i][0] + st[i][2];
      mean *= 0.125f;
      float m2 = 0.f;
#pragma unroll
      for (int i = 0; i < 4; ++i) { const float d0 = st[i][0] - mean, d1 = st[i][2] - mean; m2 += st[i][1] + st[i][3] + 64.f * (d0 * d0 + d1 * d1); }
      const float den = rbf(__builtin_sqrtf(rbf(rbf(m2 * (1.f / D)) + 1e-5f)));
      const float mb = rbf(mean);
      unsigned d0 = q == 0 ? pk(st[0][0] - mb, st[0][2] - mb) : pk(st[2][0] - mb, st[2][2] - mb);
      unsigned d1 = q == 0 ? pk(st[1][0] - mb, st[1][2] - mb) : pk(st[3][0] - mb, st[3][2] - mb);
      if (q >= 2) { d0 = q == 2 ? pk(den, 0.f) : 0u; d1 = 0u; }
      *(v4u*)(shm + L_H2 + (64 + w) * 1024 + l16) = v4u{d0, d1, d0, d1};
      if (q == 0) ((float*)(shm + L_RSTD))[w * 16 + m] = 1.f / den;
      WAIT_L0(); BAR(); SCHED();
    }
    STAMP_MID(6)
    {
      constexpr int WW = 8, WIN = 2;
      const int nk = w == 3 ? 10 : 11;
      const int r5 = (c + 3 * pl) % 10;
      auto kbo = [&](int g) { const int k = g + r5; return (unsigned)((k >= nk ? k - nk : k) * 1024); };
      v4u w2f[WW][4];
      const unsigned a0 = OFF_W2 + ((4 * c) * NKB_H + 11 * w) * 1024;
#pragma unroll
      for (int k = 0; k < WW; ++k)
#pragma unroll
        for (int i = 0; i < 4; ++i) w2f[k][i] = bld<0>(rs, l16, a0 + i * NKB_H * 1024 + kbo(k));
      SCHED();
      wait_flag(ws, 2, p);
      STAMP_MID(7)
      v4u hf[WIN][4];
      unsigned b0[4];
#pragma unroll
      for (int j = 0; j < 4; ++j) b0[j] = OFF_H1 + ((4 * p + ((j + w) & 3)) * NKB_H + 11 * w) * 1024;
#pragma unroll
      for (int g = 0; g < WIN; ++g)
#pragma unroll
        for (int j = 0; j < 4; ++j) hf[g][j] = bld<SC1>(rs, l16, b0[j] + kbo(g));
      SCHED();
#pragma unroll
      for (int g = 0; g < 11; ++g) {
        if (g < 10 || nk == 11) {
#pragma unroll
          for (int j = 0; j < 4; ++j) {
            const v4u cur = hf[g % WIN][j];
#pragma unroll
            for (int i = 0; i < 4; ++i) acc[i][j] = mfma(w2f[g % WW][i], cur, g == 0 ? f32x4{0, 0, 0, 0} : acc[i][j]);
            if (g + WIN < 11) hf[g % WIN][j] = bld<SC1>(rs, l16, b0[j] + kbo(g + WIN));
            SCHED();
          }
          if (g + WW < 11) {
#pragma unroll
            for (int i = 0; i < 4; ++i) w2f[g % WW][i] = bld<0>(rs, l16, a0 + i * NKB_H * 1024 + kbo(g + WW));
            SCHED();
          }
        }
      }
      reduce_gate(modl + 2560 + c * 64);
#pragma unroll
      for (int ip = 0; ip < 2; ++ip) bst<0>(RSRC2(out), (l16 & 0xf0) * 64 + (l16 >> 8) * 16 + ip * 64, (p * 64 + w * 16) * 1024 + c * 128, res[ip]);
    }
    if (w == 0) STAMP_END(2, b, ws)
    return;
  }

  const int wr = w >> 1, wc = w & 1;
  {
    f32x4 pa[3], pb[3];
#pragma unroll
    for (int i = 0; i < 3; ++i) {
      pa[i] = __builtin_bit_cast(f32x4, bld<0>(rs, tid * 16, OFF_MOD + i * 4096));
      pb[i] = __builtin_bit_cast(f32x4, bld<0>(rs, tid * 16, OFF_MOD + 12288 + i * 4096));
    }
    if (tid < 32) ((unsigned*)(shm + L_ZERO))[tid] = 0u;
#pragma unroll
    for (int i = 0; i < 3; ++i) {
      const int sec = 2 * i + (w >> 1);
      const float one = (sec == 1 || sec == 4) ? 1.f : 0.f;
      const f32x4 t = pa[i] + pb[i];
      const unsigned u0 = pk(t[0], t[1]), u1 = pk(t[2], t[3]);
      const unsigned z0 = pk(bflo(u0) + one, bfhi(u0) + one), z1 = pk(bflo(u1) + one, bfhi(u1) + one);
      *(f32x4*)(shm + L_MOD + i * 4096 + tid * 16) = f32x4{bflo(z0), bfhi(z0), bflo(z1), bfhi(z1)};
      *(v2u*)(shm + L_MB + i * 2048 + tid * 8) = v2u{z0, z1};
    }
  }
  {
    const int xoff = q * 2048 + (wc * 64 + m) * 16;
    int wo[2];
#pragma unroll
    for (int kk = 0; kk < 2; ++kk) wo[kk] = 16384 + wr * 8192 + m * 128 + (((4 * kk + q) ^ ((m >> 1) & 7)) * 16);
    f32x4 acc[4][4], rsum = {0, 0, 0, 0};
#pragma unroll
    for (int i = 0; i < 4; ++i)
#pragma unroll
      for (int j = 0; j < 4; ++j) acc[i][j] = f32x4{0, 0, 0, 0};
    const v4u ones = {0x3f803f80u, 0x3f803f80u, 0x3f803f80u, 0x3f803f80u};
    v4u XA[4], WA[4], XB[4], WB[4];
    int sA = 0;
    auto rd = [&](int kk, v4u (&X)[4], v4u (&W)[4]) {
      W[0] = *(const v4u*)(shm + sA + wo[kk]);
      W[1] = *(const v4u*)(shm + sA + wo[kk] + 2048);
      W[2] = *(const v4u*)(shm + sA + wo[kk] + 6144 - wc * 2048);
      W[3] = *(const v4u*)(shm + sA + wo[kk] + 4096 + wc * 2048);
#pragma unroll
      for (int j = 0; j < 4; ++j) X[j] = *(const v4u*)(shm + sA + xoff + kk * 8192 + j * 256);
      if (kk) sA = (sA + STAGE) & (NS * STAGE - 1);
    };
    auto mm = [&](v4u (&X)[4], v4u (&W)[4]) {
#pragma unroll
      for (int j = 0; j < 4; ++j)
#pragma unroll
        for (int i = 0; i < 4; ++i) acc[i][j] = mfma(X[i], W[j], acc[i][j]);
      rsum = mfma(ones, W[3], rsum);
    };
    auto mix = [&]() {
#pragma unroll
      for (int i = 0; i < 8; ++i) {
        __builtin_amdgcn_sched_group_barrier(0x008, 1, 0);
        __builtin_amdgcn_sched_group_barrier(0x100, 1, 0);
      }
      __builtin_amdgcn_sched_group_barrier(0x008, 9, 0);
    };
    WAIT_L0();
    BAR();
    STAMP_MID(0)
    rd(0, XA, WA);
#pragma unroll 1
    for (int t = 0; t < 15; ++t) {
      SCHED();
      rd(1, XB, WB); mm(XA, WA); mix();
      SCHED();
      WAIT_L0();
      BAR();
      SCHED();
      rd(0, XA, WA); mm(XB, WB); mix();
    }
    SCHED();
    rd(1, XB, WB); mm(XA, WA); mix();
    SCHED();
    mm(XB, WB);
    STAMP_MID(1)
    const float* ml = modl + 512 + dq * 128 + wc * 64;
#pragma unroll
    for (int jh = 0; jh < 2; ++jh) {
      const int gq = (2 * wr + jh + dq + 1) & 3;
      const float sg = (gq >> 1) ? -1.f : 1.f;
#pragma unroll
      for (int ip = 0; ip < 2; ++ip) {
        const f32x4 c0 = *(const f32x4*)(ml + 32 * ip + 8 * q) * sg, c1 = *(const f32x4*)(ml + 32 * ip + 8 * q + 4) * sg;
#pragma unroll
        for (int jl = 0; jl < 2; ++jl) {
          const int j = 2 * jh + jl;
          const int u = j < 2 ? j : (j == 3 ? wc : 1 - wc);
          const f32x4 a0 = acc[2 * ip][j] * c0, a1 = acc[2 * ip + 1][j] * c1;
          v4u o; o.x = pk(a0[0], a0[1]); o.y = pk(a0[2], a0[3]); o.z = pk(a1[0], a1[1]); o.w = pk(a1[2], a1[3]);
          const int tt = 4 * p + 2 * (gq & 1) + u, kb = (gq >> 1) * 16 + 4 * dq + 2 * wc + ip;
          bst<SC1>(rs, l16, OFF_PQ + ((tt * 32 + kb) * 2 + kh) * 1024, o);
        }
      }
    }
    if (wr == 1 && q == 0) {
      const float r = (dq >> 1) ? -rsum[0] : rsum[0];
      __builtin_amdgcn_raw_buffer_store_b32(__builtin_bit_cast(unsigned, r), rs, m * 32,
                                            OFF_RS + (p * 2 + (dq >> 1)) * 512 + ((2 * (dq & 1) + wc) * 2 + kh) * 4, SC1);
    }
  }
  WAIT_V(0);
  signal(shm, 0, ws, p, lane);
  STAMP_MID(2)
  SCHED();
  const int pr0 = c < 3 ? 6 * c : 18 + 5 * (c - 3);
  const bool has3 = (c < 3) || (w < 2);
  const int ft2 = has3 ? 2 * (pr0 + 4 + (w >> 1)) + (w & 1) : 2 * (pr0 + w);
  constexpr int WK = 12;
  v4u w1f[WK][3];
  f32x4 accU[3];
#pragma unroll
  for (int i = 0; i < 3; ++i) accU[i] = f32x4{0, 0, 0, 0};
  const unsigned a01 = OFF_W1 + (2 * (pr0 + w)) * NKB_X * 1024, a2 = OFF_W1 + ft2 * NKB_X * 1024;
  auto ldw1 = [&](int k) {
    const unsigned ko = ((k + r4) & 15) * 1024;
    w1f[k % WK][0] = bld<0>(rs, l16, a01 + ko); w1f[k % WK][1] = bld<0>(rs, l16, a01 + NKB_X * 1024 + ko); w1f[k % WK][2] = bld<0>(rs, l16, a2 + ko);
  };
  auto upd = [&](int k) {
    const int kb = (k + r4) & 15;
    const int ad = m == 8 ? L_MB + (1536 + kb * 32 + q * 8) * 2 : (m == (kb >> 1) ? L_MB + (2048 + kb * 32 + q * 8) * 2 : L_ZERO);
    const v4u cf = *(const v4u*)(shm + ad);
#pragma unroll
    for (int i = 0; i < 3; ++i) accU[i] = mfma(cf, w1f[k % WK][i], accU[i]);
  };
  ldw1(0);
#pragma unroll
  for (int k = 1; k < WK; ++k) {
    if (k == W1_NB1) { upd(k - 1); SCHED(); BAR(); SCHED(); ldw1(k); }
    else { ldw1(k); SCHED(); upd(k - 1); }
    SCHED();
  }
  upd(WK - 1);
  SCHED();
  f32x4 acc[3][4];
  v4u hb[2][4];
  auto rdh = [&](int k) {
#pragma unroll
    for (int j = 0; j < 4; ++j) hb[k & 1][j] = *(const v4u*)(shm + L_H2 + (k * 4 + j) * 1024 + l16);
  };
  BAR();
  SCHED();
  STAMP_MID(3)
  rdh(0);
#pragma unroll
  for (int k = 0; k < 16; ++k) {
    if (k == 3) { BAR(); }
    SCHED();
    rdh(k + 1);
    SCHED();
    if (k >= WK) upd(k);
#pragma unroll
    for (int j = 0; j < 4; ++j)
#pragma unroll
      for (int i = 0; i < 3; ++i) acc[i][j] = mfma(w1f[k % WK][i], hb[k & 1][j], k == 0 ? f32x4{0, 0, 0, 0} : acc[i][j]);
    if (k + WK < 16) ldw1(k + WK);
    SCHED();
  }
#pragma unroll
  for (int i = 0; i < 3; ++i) {
    const unsigned h0 = pk(accU[i][0], accU[i][1]), h1 = pk(accU[i][2], accU[i][3]);
    const v4u uf = {h0, h1, pk(accU[i][0] - bflo(h0), accU[i][1] - bfhi(h0)), pk(accU[i][2] - bflo(h1), accU[i][3] - bfhi(h1))};
#pragma unroll
    for (int j = 0; j < 4; ++j) acc[i][j] = mfma(uf, hb[0][j], acc[i][j]);
  }
  STAMP_MID(4)
#pragma unroll
  for (int j = 0; j < 4; ++j) {
    const float rsd = ((const float*)(shm + L_RSTD))[j * 16 + m];
    const f32x4 a0 = acc[0][j] * rsd, a1 = acc[1][j] * rsd, a2v = acc[2][j] * rsd;
    v4u o;
    o.x = pk(fmaxf(a0[0], 0.f), fmaxf(a0[1], 0.f)); o.y = pk(fmaxf(a0[2], 0.f), fmaxf(a0[3], 0.f));
    o.z = pk(fmaxf(a1[0], 0.f), fmaxf(a1[1], 0.f)); o.w = pk(fmaxf(a1[2], 0.f), fmaxf(a1[3], 0.f));
    bst<SC1>(rs, l16, OFF_H1 + ((4 * p + j) * NKB_H + pr0 + w) * 1024, o);
    if (has3) {
      v2u o2 = {pk(fmaxf(a2v[0], 0.f), fmaxf(a2v[1], 0.f)), pk(fmaxf(a2v[2], 0.f), fmaxf(a2v[3], 0.f))};
      __builtin_amdgcn_raw_buffer_store_b64(o2, rs, l16, OFF_H1 + ((4 * p + j) * NKB_H + (ft2 >> 1)) * 1024 + (ft2 & 1) * 8, SC1);
    }
  }
  WAIT_V(0);
  signal(shm, 2, ws, p, lane);
  STAMP_MID(5)
  if (w == 0) STAMP_END(1, b, ws)
}

extern "C" void kernel_launch(void* const* d_in, const int* in_sizes, int n_in, void* d_out, int out_size, void* d_ws, size_t ws_size,
                              hipStream_t stream) {
  const char* x = (const char*)d_in[0];
  const char* wsr = (const char*)d_in[1]; const char* wsi = (const char*)d_in[2];
  const char* wdr = (const char*)d_in[3]; const char* wdi = (const char*)d_in[4];
  const char* w1 = (const char*)d_in[6]; const char* w2 = (const char*)d_in[7];
  const char* c = (const char*)d_in[8]; const char* wm = (const char*)d_in[9];
  char* ws = (char*)d_ws;
  (void)hipFuncSetAttribute((const void*)k2_mega, hipFuncAttributeMaxDynamicSharedMemorySize, K2_LDS);
  k1_prep<<<K1_GRID, 512, 0, stream>>>(x, wsr, wsi, wdr, wdi, w1, w2, c, wm, ws);
  k2_mega<<<256, 512, K2_LDS, stream>>>(x, wsr, wsi, ws, (char*)d_out);
}
```

```cpp
#include <hip/hip_runtime.h>
#include <hip/hip_bf16.h>
#include <stdint.h>

typedef unsigned v4u __attribute__((ext_vector_type(4)));
typedef unsigned v2u __attribute__((ext_vector_type(2)));
typedef float f32x4 __attribute__((ext_vector_type(4)));
typedef float f32x2 __attribute__((ext_vector_type(2)));
typedef short bf16x8 __attribute__((ext_vector_type(8)));
typedef __bf16 bf2 __attribute__((ext_vector_type(2)));
#define GAS __attribute__((address_space(1)))
#define LAS __attribute__((address_space(3)))
#define WAIT_V(n) asm volatile("s_waitcnt vmcnt(%0)" ::"n"(n) : "memory")
#define CFENCE() asm volatile("" ::: "memory")
#define WAIT_L0() do { CFENCE(); __builtin_amdgcn_s_waitcnt(0xC07F); } while (0)
#define WAIT_V0() do { CFENCE(); __builtin_amdgcn_s_waitcnt(0x0F70); CFENCE(); } while (0)
#define SCHED() __builtin_amdgcn_sched_barrier(0)
#define BAR() do { CFENCE(); __builtin_amdgcn_s_barrier(); CFENCE(); } while (0)
#define RSRC(p, n) __builtin_amdgcn_make_buffer_rsrc((void*)(p), 0, (n), 0x00020000)
constexpr int SC1 = 16, NTP = 2;

#define STAMP_BEGIN()
#define STAMP_MID(i) {}
#define STAMP_END(k, blk, ws) {}

constexpr int D = 512, F = 1376;
constexpr int NKB_X = 16, NKB_H = 43;
constexpr unsigned OFF_EPOCH = 0;
#define FSTR 256
constexpr unsigned OFF_FL   = 24u << 20;
constexpr unsigned LN_G1 = 0;
constexpr unsigned LN_G2 = 64;
constexpr unsigned LN_F1 = 128;
constexpr unsigned LN_F3 = 160;
constexpr unsigned LN_F2 = 288;
constexpr unsigned LN_END = 416;
#define FLINE(l) (OFF_FL + (l) * FSTR)
constexpr unsigned OFF_MOD  = 49152;
constexpr unsigned OFF_RS   = 131072;
constexpr unsigned OFF_ST   = 196608;
constexpr unsigned OFF_XNT  = 524288;
constexpr unsigned OFF_WD   = OFF_XNT + (2u << 20);
constexpr unsigned OFF_W1   = OFF_WD + (1u << 20);
constexpr unsigned OFF_W2   = OFF_W1 + 86 * 16 * 1024;
constexpr unsigned OFF_PQ   = OFF_W2 + 32 * 43 * 1024;
constexpr unsigned OFF_X1   = OFF_PQ + (8u << 20);
constexpr unsigned OFF_H1   = OFF_X1 + (2u << 20);
constexpr unsigned WS_BYTES = FLINE(LN_END);

__device__ __forceinline__ float asf(unsigned u) { return __builtin_bit_cast(float, u); }
__device__ __forceinline__ unsigned asu(float f) { return __builtin_bit_cast(unsigned, f); }
__device__ __forceinline__ float bflo(unsigned u) { return asf(u << 16); }
__device__ __forceinline__ float bfhi(unsigned u) { return asf(u & 0xffff0000u); }
__device__ __forceinline__ unsigned pk(float lo, float hi) {
  f32x2 v = {lo, hi};
  return __builtin_bit_cast(unsigned, __builtin_convertvector(v, bf2));
}
__device__ __forceinline__ float rbf(float x) { return bflo(pk(x, x)); }
__device__ __forceinline__ f32x4 mfma(v4u a, v4u b, f32x4 c) {
  return __builtin_amdgcn_mfma_f32_16x16x32_bf16(__builtin_bit_cast(bf16x8, a), __builtin_bit_cast(bf16x8, b), c, 0, 0, 0);
}
template <int CTRL> __device__ __forceinline__ float dpp_add(float x) {
  int y = __builtin_amdgcn_update_dpp(0, __builtin_bit_cast(int, x), CTRL, 0xf, 0xf, false);
  return x + __builtin_bit_cast(float, y);
}
__device__ __forceinline__ float wave_sum(float x) {
  x = dpp_add<0x111>(x); x = dpp_add<0x112>(x); x = dpp_add<0x114>(x); x = dpp_add<0x118>(x);
  x = dpp_add<0x142>(x); x = dpp_add<0x143>(x);
  return __builtin_bit_cast(float, __builtin_amdgcn_readlane(__builtin_bit_cast(int, x), 63));
}
__device__ __forceinline__ float quad16_sum(float x) {
  float y = x;
  asm volatile("s_nop 1\n\tv_permlane16_swap_b32 %0, %1" : "+v"(x), "+v"(y));
  x += y;
  y = x;
  asm volatile("s_nop 1\n\tv_permlane32_swap_b32 %0, %1" : "+v"(x), "+v"(y));
  return x + y;
}
__device__ __forceinline__ int fresh_lane() { int l; asm volatile("v_mbcnt_lo_u32_b32 %0, -1, 0\n\tv_mbcnt_hi_u32_b32 %0, -1, %0" : "=v"(l)); return l; }
__device__ __forceinline__ v4u ldnt(const void* p) { return __builtin_nontemporal_load((const v4u*)p); }
template <int AUX> __device__ __forceinline__ v4u bld(__amdgpu_buffer_rsrc_t rs, unsigned voff, unsigned soff) { return __builtin_amdgcn_raw_buffer_load_b128(rs, voff, soff, AUX); }
template <int AUX> __device__ __forceinline__ void bst(__amdgpu_buffer_rsrc_t rs, unsigned voff, unsigned soff, v4u v) { __builtin_amdgcn_raw_buffer_store_b128(v, rs, voff + soff, 0, AUX); }
__device__ __forceinline__ void st1(__amdgpu_buffer_rsrc_t rs, unsigned voff, unsigned soff, unsigned v) { __builtin_amdgcn_raw_buffer_store_b32(v, rs, voff, soff, SC1); }
#define FLAG_MODE 1
__device__ __forceinline__ void flag_set(char* ws, __amdgpu_buffer_rsrc_t rs, unsigned off, unsigned v) {
  asm volatile("global_atomic_swap %0, %1, %2 sc1" :: "v"(off), "v"(v), "s"(ws) : "memory");
}
__device__ __forceinline__ unsigned ld1(__amdgpu_buffer_rsrc_t rs, unsigned voff, unsigned soff) { return __builtin_amdgcn_raw_buffer_load_b32(rs, voff, soff, SC1); }
template <int AUX> __device__ __forceinline__ void glds16(const char* g, LAS char* l) {
  __builtin_amdgcn_global_load_lds((const GAS unsigned*)g, (LAS unsigned*)l, 16, 0, AUX);
}

#define AUX_X 16
#define WD_A 4
#define WD_B 8
#define W1_NB1 8
#define W1_AH 2
#define POLL_SLEEP 2
constexpr int NS = 4, STAGE = 32768;
constexpr int L_TOP = NS * STAGE;
constexpr int L_MOD = L_TOP;
constexpr int L_MB = L_MOD + 12288;
constexpr int L_ZERO = L_TOP + 24576;
constexpr int L_CNT = L_ZERO + 64;
constexpr int L_RSTD = L_CNT + 64;
constexpr int K_LDS = L_RSTD + 256;
constexpr int L_LN = 3 * STAGE;
constexpr int L_H2 = 0, H2W = 17 * 1024;
constexpr int L_W1X = 4 * H2W;
constexpr unsigned SPIN_CAP = 1u << 16;

__device__ __forceinline__ void wait_words(__amdgpu_buffer_rsrc_t rs, unsigned voff, unsigned soff, unsigned tag, bool sel) {
#pragma unroll 1
  for (unsigned spins = 0; spins < SPIN_CAP; ++spins) {
    CFENCE();
    const unsigned v = ld1(rs, voff, soff);
    if (__builtin_amdgcn_ballot_w64(sel && v != tag) == 0ull) break;
    __builtin_amdgcn_s_sleep(POLL_SLEEP);
  }
  __builtin_amdgcn_fence(__ATOMIC_ACQUIRE, "wavefront");
  CFENCE();
  SCHED();
}
__device__ __forceinline__ void wait_lds(volatile LAS unsigned* p, unsigned n) {
#pragma unroll 1
  for (unsigned spins = 0; spins < SPIN_CAP * 64; ++spins) {
    if (__builtin_amdgcn_readfirstlane(*p) >= n) break;
    __builtin_amdgcn_s_sleep(1);
  }
  CFENCE();
  SCHED();
}
__device__ __forceinline__ unsigned lds_inc(volatile LAS unsigned* p, int lane) {
  unsigned old = 0;
  if (lane == 0) old = __hip_atomic_fetch_add((LAS unsigned*)p, 1u, __ATOMIC_RELAXED, __HIP_MEMORY_SCOPE_WORKGROUP);
  return __builtin_amdgcn_readfirstlane(old);
}

__global__ __launch_bounds__(512, 2) void fused_all(const char* __restrict__ x, const char* __restrict__ wsr, const char* __restrict__ wsi,
                                                    const char* __restrict__ wdr, const char* __restrict__ wdi,
                                                    const char* __restrict__ w1, const char* __restrict__ w2, const char* __restrict__ cvec,
                                                    const char* __restrict__ wmod, char* __restrict__ ws, char* __restrict__ out) {
  extern __shared__ __attribute__((aligned(16))) char shm[];
  STAMP_BEGIN()
  const int b = blockIdx.x, xcd = b & 7, idx = b >> 3, pl = idx >> 3, p = xcd * 4 + pl, c = idx & 7, dq = c & 3, kh = c >> 2;
  const int tid = threadIdx.x, lane = tid & 63, w8 = __builtin_amdgcn_readfirstlane(tid >> 6), w = w8 & 3;
  const int m = lane & 15, q = lane >> 4;
  const __amdgpu_buffer_rsrc_t rs = RSRC(ws, WS_BYTES);
  const float* modl = (const float*)(shm + L_MOD);
  const unsigned l16 = lane * 16;
  volatile LAS unsigned* cnt = (volatile LAS unsigned*)((LAS char*)shm + L_CNT);

  const v4u xr = ldnt(x + (size_t)(b * 8 + w8) * 1024 + l16);
  const unsigned tagv = ld1(rs, 0, OFF_EPOCH);
  auto ln_publish = [&]() -> unsigned {
    float v[8];
#pragma unroll
    for (int u = 0; u < 4; ++u) { v[2 * u] = bflo(xr[u]); v[2 * u + 1] = bfhi(xr[u]); }
    const float mean = wave_sum(((v[0] + v[1]) + (v[2] + v[3])) + ((v[4] + v[5]) + (v[6] + v[7]))) * (1.f / D);
    float ss = 0.f;
#pragma unroll
    for (int j = 0; j < 8; ++j) { const float t = v[j] - mean; ss += t * t; }
    const float var = wave_sum(ss) * (1.f / D);
    const float mb = rbf(mean);
    const float rsd = 1.f / rbf(__builtin_sqrtf(rbf(rbf(var) + 1e-5f)));
    v4u o;
#pragma unroll
    for (int u = 0; u < 4; ++u) {
      const unsigned d2 = pk(v[2 * u] - mb, v[2 * u + 1] - mb);
      o[u] = pk(bflo(d2) * rsd, bfhi(d2) * rsd);
    }
    *(v4u*)(shm + L_LN + w8 * 1024 + l16) = o;
    if (tid < 32) ((unsigned*)(shm + L_ZERO))[tid] = 0u;
    WAIT_L0();
    BAR();
    const int d = (tid & ~31) + 8 * ((tid >> 2) & 3) + 4 * ((tid >> 4) & 1) + (tid & 3);
    const unsigned short* sp = (const unsigned short*)(shm + L_LN) + d;
    v4u t;
#pragma unroll
    for (int u = 0; u < 4; ++u) t[u] = (unsigned)sp[(2 * u) * 512] | ((unsigned)sp[(2 * u + 1) * 512] << 16);
    bst<SC1>(rs, tid * 16, OFF_XNT + b * 8192, t);
    const unsigned tag = __builtin_amdgcn_readfirstlane(tagv) + 1u;
    WAIT_V0();
    if (lds_inc(cnt + 0, lane) == 7u) { if (lane < 8) flag_set(ws, rs, FLINE(LN_G1 + lane * 8 + (b >> 5)) + (b & 31) * 4, tag); }
    return tag;
  };

  if (w8 >= 4) {
    v4u wd[8][4], res[2];
    unsigned tag;
    {
      const int gq = (w + dq + 1) & 3;
      const char* wbase = (gq < 2 ? wsr : wsi) + (size_t)(p * 64 + (gq & 1) * 32 + (lane >> 3)) * 4096 + kh * 2048;
      const int sl = lane & 7, h = lane >> 4;
      const char* wsrc0 = wbase + ((sl ^ h) * 16);
      const char* wsrc1 = wbase + ((sl ^ (4 + h)) * 16) + 8 * 4096;
      const char* xsrc = ws + OFF_XNT + ((size_t)(kh * 128 + 2 * w) * 512 + dq * 128 + lane) * 16;
      auto issW = [&](int st) {
        LAS char* dst = (LAS char*)shm + (st & 3) * STAGE + w * 4096 + 16384;
#pragma unroll
        for (int i = 0; i < 4; ++i) glds16<NTP>(((i & 1) ? wsrc1 : wsrc0) + (size_t)(i >> 1) * 16 * 4096 + st * 128, dst + i * 1024);
      };
      auto issX = [&](int st) {
        LAS char* dst = (LAS char*)shm + (st & 3) * STAGE + w * 4096;
#pragma unroll
        for (int i = 0; i < 4; ++i) glds16<AUX_X>(xsrc + (size_t)st * 65536 + (i >> 1) * 8192 + (i & 1) * 1024, dst + i * 1024);
      };
      tag = ln_publish();
      STAMP_MID(13)
      issW(0); issW(1); issW(2); issW(3);
#pragma unroll
      for (int ip = 0; ip < 2; ++ip) res[ip] = bld<NTP>(RSRC(x, 2048 * 1024), m * 1024 + q * 16 + ip * 64, (p * 64 + w * 16) * 1024 + c * 128);
      SCHED();
      if (w < 2) { wait_words(rs, (lane >> 5) * FSTR + (lane & 31) * 4, FLINE(LN_G1 + xcd * 8 + kh * 4 + w * 2), tag, true); lds_inc(cnt + 1, lane); }
      wait_lds(cnt + 1, 2);
      wait_lds(cnt + 0, 8);
      STAMP_MID(14)
      issX(0); issX(1); issX(2); issX(3);
      WAIT_V(12); BAR();
      WAIT_V(8); BAR();
      issX(4);
      WAIT_V(8); BAR();
      issW(4); issW(5);
      WAIT_V(12); BAR();
      issX(5); issX(6);
#pragma unroll 1
      for (int t = 3; t < 11; t += 2) {
        WAIT_V(12); BAR();
        issW(t + 3); issW(t + 4);
        WAIT_V(12); BAR();
        issX(t + 4); issX(t + 5);
      }
      WAIT_V(12); BAR();
      issW(14); issW(15);
      STAMP_MID(0)
      const unsigned a0 = OFF_WD + ((4 * c) * 32 + 8 * w) * 1024;
      auto ldwd = [&](int f0, int f1) {
        SCHED();
#pragma unroll
        for (int f = f0; f < f1; ++f) wd[f >> 2][f & 3] = bld<SC1>(rs, l16, a0 + ((f & 3) * 32 + (f >> 2)) * 1024);
        SCHED();
      };
      WAIT_V(12); BAR();
      issX(15);
      ldwd(0, WD_A);
      WAIT_V(8 + WD_A); BAR();
      ldwd(WD_A, WD_A + WD_B);
      WAIT_V(WD_A + WD_B); BAR();
      ldwd(WD_A + WD_B, 32);
    }
    SCHED();
    f32x4 acc[4][4];
    f32x4* red = (f32x4*)(shm + L_H2);
    auto put = [&](int j, int ln) {
#pragma unroll
      for (int i = 0; i < 4; ++i) red[((j + w) & 3) * (H2W / 16) + ((j - 1) * 4 + i) * 64 + ln] = acc[i][j];
    };
    {
      f32x4 accS[4];
      v4u sf[8];
#pragma unroll
      for (int k = 0; k < 8; ++k) sf[k] = *(const v4u*)(shm + L_MB + ((8 * (w & 1) + k) * 32 + q * 8) * 2);
      SCHED();
#pragma unroll
      for (int k = 0; k < 8; ++k)
#pragma unroll
        for (int i = 0; i < 4; ++i) accS[i] = mfma(wd[k][i], sf[k], k == 0 ? f32x4{0, 0, 0, 0} : accS[i]);
      SCHED();
      STAMP_MID(1)
      if (w == 0) { wait_words(rs, (lane & 31) * 4, FLINE(LN_F1 + p), tag, true); lds_inc(cnt + 4, lane); }
      else wait_lds(cnt + 4, 1);
      STAMP_MID(2)
      f32x2 rr[4];
#pragma unroll
      for (int j = 0; j < 4; ++j)
#pragma unroll
        for (int h = 0; h < 2; ++h) rr[j][h] = asf(ld1(rs, m * 4 + h * 128, OFF_RS + (p * 2 + (w >> 1)) * 1024 + ((j + w) & 3) * 256));
      constexpr int WIN = 2;
      v4u pq[WIN][4];
      unsigned b0[4];
#pragma unroll
      for (int j = 0; j < 4; ++j) b0[j] = OFF_PQ + ((4 * p + ((j + w) & 3)) * 64 + 16 * w) * 1024;
#pragma unroll
      for (int g = 0; g < WIN; ++g)
#pragma unroll
        for (int jj = 0; jj < 4; ++jj) { const int j = 3 - jj; pq[g][j] = bld<SC1>(rs, l16, b0[j] + g * 1024); }
      SCHED();
#pragma unroll
      for (int g = 0; g < 16; ++g) {
#pragma unroll
        for (int jj = 0; jj < 4; ++jj) {
          const int j = 3 - jj;
          const v4u cur = pq[g % WIN][j];
#pragma unroll
          for (int i = 0; i < 4; ++i) acc[i][j] = mfma(wd[g >> 1][i], cur, g == 0 ? accS[i] * (rr[j][0] + rr[j][1]) : acc[i][j]);
          if (g + WIN < 16) pq[g % WIN][j] = bld<SC1>(rs, l16, b0[j] + (g + WIN) * 1024);
          if (g == 15 && j < 3) put(j + 1, lane);
          SCHED();
        }
      }
    }
    STAMP_MID(3)
    auto reduce_gate = [&](const float* g, int ln) {
      const int q = ln >> 4;
      f32x4 gv[4];
#pragma unroll
      for (int i = 0; i < 4; ++i) gv[i] = *(const f32x4*)(g + 32 * (i >> 1) + 8 * q + 4 * (i & 1));
      WAIT_L0();
      BAR();
      SCHED();
      f32x4 pt[3][4];
#pragma unroll
      for (int i = 0; i < 4; ++i)
#pragma unroll
        for (int s = 0; s < 3; ++s) pt[s][i] = red[w * (H2W / 16) + (s * 4 + i) * 64 + ln];
      SCHED();
#pragma unroll
      for (int i = 0; i < 4; ++i) {
        const f32x4 t = ((acc[i][0] + pt[0][i]) + (pt[1][i] + pt[2][i])) * gv[i];
        const unsigned r0 = res[i >> 1][2 * (i & 1)], r1 = res[i >> 1][2 * (i & 1) + 1];
        res[i >> 1][2 * (i & 1)] = pk(bflo(r0) + t[0], bfhi(r0) + t[1]);
        res[i >> 1][2 * (i & 1) + 1] = pk(bflo(r1) + t[2], bfhi(r1) + t[3]);
      }
    };
    const unsigned xb = OFF_X1 + (4 * p + w) * NKB_X * 1024, sb0 = OFF_ST + (p * 4 + w) * 1024;
    {
      f32x4 cv[4];
#pragma unroll
      for (int i = 0; i < 4; ++i) cv[i] = *(const f32x4*)(modl + 2048 + c * 64 + 32 * (i >> 1) + 8 * q + 4 * (i & 1));
      reduce_gate(modl + 1024 + c * 64, lane);
      STAMP_MID(10)
      float v[16];
#pragma unroll
      for (int u = 0; u < 8; ++u) { v[2 * u] = bflo(res[u >> 2][u & 3]); v[2 * u + 1] = bfhi(res[u >> 2][u & 3]); }
      float s = 0.f;
#pragma unroll
      for (int u = 0; u < 16; u += 4) s += (v[u] + v[u + 1]) + (v[u + 2] + v[u + 3]);
      const float mc = quad16_sum(s) * (1.f / 64);
#pragma unroll
      for (int u = 0; u < 16; ++u) v[u] -= mc;
#pragma unroll
      for (int ip = 0; ip < 2; ++ip) {
        v4u o;
#pragma unroll
        for (int u = 0; u < 4; ++u) o[u] = pk(v[8 * ip + 2 * u] * cv[2 * ip + (u >> 1)][(2 * u) & 3], v[8 * ip + 2 * u + 1] * cv[2 * ip + (u >> 1)][(2 * u + 1) & 3]);
        bst<SC1>(rs, l16, xb + (2 * c + ip) * 1024, o);
        *(v4u*)(shm + L_H2 + w * H2W + ip * 1024 + l16) = o;
      }
      float ss = 0.f, s2 = 0.f;
#pragma unroll
      for (int u = 0; u < 16; u += 2) { ss += v[u] * v[u]; s2 += v[u + 1] * v[u + 1]; }
      ss = quad16_sum(ss + s2);
      if (q == 0) __builtin_amdgcn_raw_buffer_store_b64(v2u{asu(mc), asu(ss)}, rs, m * 8, sb0 + c * 128, SC1);
      STAMP_MID(11)
      WAIT_V0();
      if (lane == 0) flag_set(ws, rs, FLINE(LN_F2 + p * 4 + w) + c * 4, tag);
    }
    asm volatile("" : "+v"(res[0]), "+v"(res[1]));
    SCHED();
    STAMP_MID(4)
    wait_words(rs, (lane & 7) * 4, FLINE(LN_F2 + p * 4 + w), tag, true);
    STAMP_MID(5)
    {
      v4u X[14];
      v2u st[8];
#pragma unroll
      for (int k = 0; k < 14; ++k) X[k] = bld<SC1>(rs, l16, xb + ((2 * c + 2 + k) & 15) * 1024);
#pragma unroll
      for (int i = 0; i < 8; ++i) st[i] = __builtin_amdgcn_raw_buffer_load_b64(rs, m * 8 + i * 128, sb0, SC1);
      SCHED();
#pragma unroll
      for (int k = 0; k < 14; ++k) {
        *(v4u*)(shm + L_H2 + w * H2W + (2 + k) * 1024 + l16) = X[k];
        if (k == 1) { WAIT_L0(); BAR(); SCHED(); }
      }
      float mu[8], mean = 0.f, m2 = 0.f;
#pragma unroll
      for (int i = 0; i < 8; ++i) { mu[i] = asf(st[i].x); mean += mu[i]; m2 += asf(st[i].y); }
      mean *= 0.125f;
      float dv = 0.f;
#pragma unroll
      for (int i = 0; i < 8; ++i) { const float d0 = mu[i] - mean; dv += d0 * d0; }
      m2 += 64.f * dv;
      const float den = rbf(__builtin_sqrtf(rbf(rbf(m2 * (1.f / D)) + 1e-5f)));
      const float mb = rbf(mean);
      unsigned d0 = q == 0 ? pk(mu[0] - mb, mu[1] - mb) : pk(mu[4] - mb, mu[5] - mb);
      unsigned d1 = q == 0 ? pk(mu[2] - mb, mu[3] - mb) : pk(mu[6] - mb, mu[7] - mb);
      if (q >= 2) { d0 = q == 2 ? pk(den, 0.f) : 0u; d1 = 0u; }
      *(v4u*)(shm + L_H2 + w * H2W + 16 * 1024 + l16) = v4u{d0, d1, d0, d1};
      if (q == 0) ((float*)(shm + L_RSTD))[w * 16 + m] = 1.f / den;
      WAIT_L0(); BAR(); SCHED();
    }
    STAMP_MID(6)
    {
      constexpr int WW = 8, WIN = 2;
      const int nk = w == 3 ? 10 : 11;
      v4u w2f[WW][4];
      const unsigned a0 = OFF_W2 + ((4 * c) * NKB_H + 11 * w) * 1024;
#pragma unroll
      for (int k = 0; k < WW; ++k)
#pragma unroll
        for (int i = 0; i < 4; ++i) w2f[k][i] = bld<SC1>(rs, l16, a0 + (i * NKB_H + k) * 1024);
      SCHED();
      wait_lds(cnt + 2, 4);
      { const int ln = fresh_lane(); wait_words(rs, (ln & 7) * 4, FLINE(LN_F3 + p * 4 + w), tag, true); }
      STAMP_MID(7)
      v4u hf[WIN][4];
      unsigned b0[4];
#pragma unroll
      for (int j = 0; j < 4; ++j) b0[j] = OFF_H1 + ((4 * p + ((j + w) & 3)) * NKB_H + 11 * w) * 1024;
      const unsigned last = w == 3 ? 9 * 1024 : 10 * 1024;
      auto ldh = [&](int g, int j) { hf[g % WIN][j] = bld<SC1>(rs, l16, b0[j] + (g < 10 ? g * 1024 : last)); };
#pragma unroll
      for (int g = 0; g < WIN; ++g)
#pragma unroll
        for (int jj = 0; jj < 4; ++jj) ldh(g, 3 - jj);
      SCHED();
#pragma unroll
      for (int g = 0; g < 11; ++g) {
        if (g < 10 || nk == 11) {
#pragma unroll
          for (int jj = 0; jj < 4; ++jj) {
            const int j = 3 - jj;
            const v4u cur = hf[g % WIN][j];
#pragma unroll
            for (int i = 0; i < 4; ++i) acc[i][j] = mfma(w2f[g % WW][i], cur, g == 0 ? f32x4{0, 0, 0, 0} : acc[i][j]);
            if (g + WIN < 11) ldh(g + WIN, j);
            SCHED();
          }
          if (g + WW < 11) {
#pragma unroll
            for (int i = 0; i < 4; ++i) w2f[g % WW][i] = bld<SC1>(rs, l16, a0 + i * NKB_H * 1024 + (g + WW < 10 ? (g + WW) * 1024 : last));
            SCHED();
          }
        }
      }
      STAMP_MID(12)
      const int ln = fresh_lane();
      put(3, ln); put(2, ln); put(1, ln);
      reduce_gate(modl + 2560 + c * 64, ln);
      STAMP_MID(15)
#pragma unroll
      for (int ip = 0; ip < 2; ++ip) bst<0>(RSRC(out, 2048 * 1024), (ln & 15) * 1024 + (ln >> 4) * 16 + ip * 64, (p * 64 + w * 16) * 1024 + c * 128, res[ip]);
      if (b == 0 && w == 0 && ln == 0) st1(rs, 0, OFF_EPOCH, tag);
    }
    STAMP_END(w == 0 ? 2 : 8 + w, b, ws)
    return;
  }

  const int wr = w >> 1, wc = w & 1;
  v4u R[8];
  const int job = (b - 96) * 3 + w;
  const int kg = lane >> 3, ng = lane & 7;
  int jk0 = 0, jn0 = 0;
  if (b < 96) {
    const int chunk = b % 48, mh = b / 48;
#pragma unroll
    for (int i = 0; i < 8; ++i) R[i] = ldnt(wmod + (size_t)(mh * 256 + w * 64 + kg * 8 + i) * 6144 + chunk * 128 + ng * 16);
  } else if (w < 3) {
    const char* src; int ld;
    if (job < 352) {
      const bool is1 = job < 176;
      const int j = is1 ? job : job - 176;
      jk0 = (is1 ? (j & 7) : (j >> 3)) * 64; jn0 = (is1 ? (j >> 3) : (j & 7)) * 64;
      ld = is1 ? F * 2 : D * 2;
      const bool ok = (jk0 + kg * 8 < (is1 ? D : F)) && (jn0 + ng * 8 < (is1 ? F : D));
      src = (is1 ? w1 : w2) + (size_t)(ok ? jk0 + kg * 8 : 0) * ld + (ok ? jn0 + ng * 8 : 0) * 2;
    } else {
      const int jj = job - 352, et = jj >> 2, kb0 = (jj & 3) * 8;
      const int e = 32 * (et >> 1) + 8 * (m >> 2) + 4 * (et & 1) + (m & 3);
      src = (kb0 < 16 ? wdr : wdi) + (size_t)e * 1024 + (kb0 & 15) * 64 + q * 16;
      ld = 64;
    }
#pragma unroll
    for (int k = 0; k < 8; ++k) R[k] = ldnt(src + (size_t)k * ld);
  }
  v4u cvv = {0, 0, 0, 0};
  if (b < 96) cvv = *(const v4u*)(cvec + ((b / 48) * 256 + w * 64 + kg * 8) * 2);
  SCHED();
  const unsigned tag = ln_publish();
  STAMP_MID(13)
  if (b < 96) {
    float a8[8] = {0.f, 0.f, 0.f, 0.f, 0.f, 0.f, 0.f, 0.f};
#pragma unroll
    for (int i = 0; i < 8; ++i) {
      const float c0 = (i & 1) ? bfhi(cvv[i >> 1]) : bflo(cvv[i >> 1]);
      const float sl = rbf(c0 / (1.f + __expf(-c0)));
#pragma unroll
      for (int u = 0; u < 4; ++u) { a8[2 * u] += sl * bflo(R[i][u]); a8[2 * u + 1] += sl * bfhi(R[i][u]); }
    }
    float* red = (float*)(shm + L_TOP);
    *(f32x4*)(red + (w * 8 + kg) * 68 + ng * 8) = f32x4{a8[0], a8[1], a8[2], a8[3]};
    *(f32x4*)(red + (w * 8 + kg) * 68 + ng * 8 + 4) = f32x4{a8[4], a8[5], a8[6], a8[7]};
    WAIT_L0();
    lds_inc(cnt + 5, lane);
    wait_lds(cnt + 5, 4);
    float s = 0.f;
#pragma unroll
    for (int i = 0; i < 8; ++i) s += red[(q * 8 + i) * 68 + w * 16 + m];
    s = quad16_sum(s);
    if (q == 0) st1(rs, m * 4, OFF_MOD + ((b / 48) * 3072 + (b % 48) * 64 + w * 16) * 4, asu(s));
  } else if (w < 3) {
    if (job < 352) {
      const bool is1 = job < 176;
      char* lds = shm + L_TOP + w * 8192;
      const int a = ng & 3, qq = kg & 3;
      char* wp = lds + ((ng >> 2) * 4 + (kg >> 2)) * 1024 + (is1 ? (a >> 1) * 2048 + (qq * 16 + 8 * (a & 1)) * 16 : (qq * 16 + 4 * a) * 16);
      const int hi = is1 ? 64 : 2048;
#pragma unroll
      for (int jn = 0; jn < 8; ++jn) {
        const unsigned sel = (jn & 1) ? 0x07060302u : 0x05040100u;
        const int u = jn >> 1;
        v4u o;
        o.x = __builtin_amdgcn_perm(R[1][u], R[0][u], sel);
        o.y = __builtin_amdgcn_perm(R[3][u], R[2][u], sel);
        o.z = __builtin_amdgcn_perm(R[5][u], R[4][u], sel);
        o.w = __builtin_amdgcn_perm(R[7][u], R[6][u], sel);
        *(v4u*)(wp + (jn >> 2) * hi + (jn & 3) * 16) = o;
      }
      WAIT_L0();
      CFENCE();
      const int nkb = is1 ? NKB_X : NKB_H;
      const unsigned dst = (is1 ? OFF_W1 : OFF_W2) + ((jn0 >> 4) * nkb + (jk0 >> 5)) * 1024;
#pragma unroll
      for (int f = 0; f < 8; ++f) {
        const int ntl = f >> 1, kbl = f & 1;
        const v4u o = *(const v4u*)(lds + f * 1024 + l16);
        if (jk0 + kbl * 32 < (is1 ? D : F) && jn0 + (ntl >> 1) * 32 < (is1 ? F : D)) bst<SC1>(rs, l16, dst + (ntl * nkb + kbl) * 1024, o);
      }
    } else {
      const int jj = job - 352;
#pragma unroll
      for (int i = 0; i < 8; ++i) bst<SC1>(rs, l16, OFF_WD + (jj * 8 + i) * 1024, R[i]);
    }
  }
  SCHED();
  STAMP_MID(14)
  const int ft0 = (c < 6 ? 11 * c : 66 + 10 * (c - 6)) + 3 * w;
  const int nft = w < 3 ? 3 : (c < 6 ? 2 : 1);
  {
    const int xoff = q * 2048 + (wc * 64 + m) * 16;
    int wo[2];
#pragma unroll
    for (int kk = 0; kk < 2; ++kk) wo[kk] = 16384 + wr * 8192 + m * 128 + (((4 * kk + q) ^ ((m >> 1) & 7)) * 16);
    f32x4 acc[4][4], rsum = {0, 0, 0, 0};
#pragma unroll
    for (int i = 0; i < 4; ++i)
#pragma unroll
      for (int j = 0; j < 4; ++j) acc[i][j] = f32x4{0, 0, 0, 0};
    const v4u ones = {0x3f803f80u, 0x3f803f80u, 0x3f803f80u, 0x3f803f80u};
    v4u XA[4], WA[4], XB[4], WB[4];
    int sA = 0;
    auto rd = [&](int kk, v4u (&X)[4], v4u (&W)[4]) {
      W[0] = *(const v4u*)(shm + sA + wo[kk]);
      W[1] = *(const v4u*)(shm + sA + wo[kk] + 2048);
      W[2] = *(const v4u*)(shm + sA + wo[kk] + 6144 - wc * 2048);
      W[3] = *(const v4u*)(shm + sA + wo[kk] + 4096 + wc * 2048);
#pragma unroll
      for (int j = 0; j < 4; ++j) X[j] = *(const v4u*)(shm + sA + xoff + kk * 8192 + j * 256);
      if (kk) sA = (sA + STAGE) & (NS * STAGE - 1);
    };
    auto mm = [&](v4u (&X)[4], v4u (&W)[4]) {
#pragma unroll
      for (int j = 0; j < 4; ++j)
#pragma unroll
        for (int i = 0; i < 4; ++i) acc[i][j] = mfma(X[i], W[j], acc[i][j]);
      rsum = mfma(ones, W[3], rsum);
    };
    auto mix = [&]() {
#pragma unroll
      for (int i = 0; i < 8; ++i) {
        __builtin_amdgcn_sched_group_barrier(0x008, 1, 0);
        __builtin_amdgcn_sched_group_barrier(0x100, 1, 0);
      }
      __builtin_amdgcn_sched_group_barrier(0x008, 9, 0);
    };
    auto steps = [&](int n) {
#pragma unroll 1
      for (int t = 0; t < n; ++t) {
        SCHED();
        rd(1, XB, WB); mm(XA, WA); mix();
        SCHED();
        WAIT_L0();
        BAR();
        SCHED();
        rd(0, XA, WA); mm(XB, WB); mix();
      }
      SCHED();
    };
    BAR();
    STAMP_MID(0)
    rd(0, XA, WA);
    steps(1);
    WAIT_V0();
    if (lds_inc(cnt + 3, lane) == 3u) { if (lane < 8) flag_set(ws, rs, FLINE(LN_G2 + lane * 8 + (b >> 5)) + (b & 31) * 4, tag); }
    steps(5);
    unsigned g2 = ld1(rs, (lane >> 5) * FSTR + (lane & 31) * 4, FLINE(LN_G2 + xcd * 8 + w * 2));
    steps(1);
    if (__builtin_amdgcn_ballot_w64(g2 != tag) != 0ull) wait_words(rs, (lane >> 5) * FSTR + (lane & 31) * 4, FLINE(LN_G2 + xcd * 8 + w * 2), tag, true);
    CFENCE();
    steps(1);
    f32x4 pa[3], pb[3];
#pragma unroll
    for (int i = 0; i < 3; ++i) {
      pa[i] = __builtin_bit_cast(f32x4, bld<SC1>(rs, tid * 16, OFF_MOD + i * 4096));
      pb[i] = __builtin_bit_cast(f32x4, bld<SC1>(rs, tid * 16, OFF_MOD + 12288 + i * 4096));
    }
    steps(2);
#pragma unroll
    for (int i = 0; i < 3; ++i) {
      const int sec = 2 * i + (w >> 1);
      const float one = (sec == 1 || sec == 4) ? 1.f : 0.f;
      const f32x4 t = pa[i] + pb[i];
      const unsigned u0 = pk(t[0], t[1]), u1 = pk(t[2], t[3]);
      const unsigned z0 = pk(bflo(u0) + one, bfhi(u0) + one), z1 = pk(bflo(u1) + one, bfhi(u1) + one);
      *(f32x4*)(shm + L_MOD + i * 4096 + tid * 16) = f32x4{bflo(z0), bfhi(z0), bflo(z1), bfhi(z1)};
      *(v2u*)(shm + L_MB + i * 2048 + tid * 8) = v2u{z0, z1};
    }
    steps(5);
    rd(1, XB, WB); mm(XA, WA); mix();
    SCHED();
    mm(XB, WB);
    STAMP_MID(1)
    const float* ml = modl + 512 + dq * 128 + wc * 64;
#pragma unroll
    for (int jh = 0; jh < 2; ++jh) {
      const int gq = (2 * wr + jh + dq + 1) & 3;
      const float sg = (gq >> 1) ? -1.f : 1.f;
#pragma unroll
      for (int ip = 0; ip < 2; ++ip) {
        const f32x4 c0 = *(const f32x4*)(ml + 32 * ip + 8 * q) * sg, c1 = *(const f32x4*)(ml + 32 * ip + 8 * q + 4) * sg;
#pragma unroll
        for (int jl = 0; jl < 2; ++jl) {
          const int j = 2 * jh + jl;
          const int u = j < 2 ? j : (j == 3 ? wc : 1 - wc);
          const f32x4 a0 = acc[2 * ip][j] * c0, a1 = acc[2 * ip + 1][j] * c1;
          v4u o; o.x = pk(a0[0], a0[1]); o.y = pk(a0[2], a0[3]); o.z = pk(a1[0], a1[1]); o.w = pk(a1[2], a1[3]);
          const int tt = 4 * p + 2 * (gq & 1) + u, kb = (gq >> 1) * 16 + 4 * dq + 2 * wc + ip;
          bst<SC1>(rs, l16, OFF_PQ + ((tt * 32 + kb) * 2 + kh) * 1024, o);
        }
      }
    }
    if (wr == 1 && q < 2) st1(rs, lane * 4, OFF_RS + (p * 2 + (dq >> 1)) * 1024 + ((2 * (dq & 1) + wc) * 2 + kh) * 128, asu((dq >> 1) ? -rsum[0] : rsum[0]));
  }
  STAMP_MID(11)
  lds_inc(cnt + 6, lane);
  WAIT_V0();
  if (lane == 0) flag_set(ws, rs, FLINE(LN_F1 + p) + (c * 4 + w) * 4, tag);
  STAMP_MID(2)
  SCHED();
  constexpr int WK = 12;
  v4u w1f[WK][3];
  f32x4 accU[3];
  unsigned a1[3];
#pragma unroll
  for (int i = 0; i < 3; ++i) a1[i] = OFF_W1 + (ft0 + (i < nft ? i : 0)) * NKB_X * 1024;
  auto ldw1 = [&](int k, v4u (&d)[3]) {
    const unsigned ko = ((k + 2 * c) & 15) * 1024;
#pragma unroll
    for (int i = 0; i < 3; ++i) d[i] = bld<SC1>(rs, l16, a1[i] + ko);
  };
  auto upd = [&](int k, v4u (&d)[3], bool first) {
    const int kb = (k + 2 * c) & 15;
    const int ad = m == 8 ? L_MB + (1536 + kb * 32 + q * 8) * 2 : (m == (kb >> 1) ? L_MB + (2048 + kb * 32 + q * 8) * 2 : L_ZERO);
    const v4u cf = *(const v4u*)(shm + ad);
#pragma unroll
    for (int i = 0; i < 3; ++i) accU[i] = mfma(cf, d[i], first ? f32x4{0, 0, 0, 0} : accU[i]);
  };
  char* w1x = shm + L_W1X + w * 12288 + l16;
  auto slot = [&](int s) -> v4u (&)[3] { return w1f[s < 4 ? 8 + s : s - 4]; };
  auto pos = [&](int s) { return s < 4 ? 12 + s : s - 4; };
  constexpr int AH = W1_AH;
#pragma unroll
  for (int s = 0; s < AH; ++s) ldw1(pos(s), slot(s));
#pragma unroll
  for (int s = 0; s < 16; ++s) {
    SCHED();
    if (s + AH == W1_NB1) { BAR(); SCHED(); }
    if (s + AH < 16) ldw1(pos(s + AH), slot(s + AH));
    SCHED();
    upd(pos(s), slot(s), s == 0);
    if (s < 4) {
      if (s == 0) wait_lds(cnt + 6, 4);
#pragma unroll
      for (int i = 0; i < 3; ++i) *(v4u*)(w1x + (s * 3 + i) * 1024) = slot(s)[i];
    }
  }
  v4u uf[3];
#pragma unroll
  for (int i = 0; i < 3; ++i) {
    const unsigned h0 = pk(accU[i][0], accU[i][1]), h1 = pk(accU[i][2], accU[i][3]);
    uf[i] = v4u{h0, h1, pk(accU[i][0] - bflo(h0), accU[i][1] - bfhi(h0)), pk(accU[i][2] - bflo(h1), accU[i][3] - bfhi(h1))};
  }
  SCHED();
  STAMP_MID(10)
  f32x4 acc[3][4];
  v4u hb[2][4];
  auto rdh = [&](int k) {
#pragma unroll
    for (int j = 0; j < 4; ++j) hb[k & 1][j] = *(const v4u*)(shm + L_H2 + j * H2W + k * 1024 + l16);
  };
  BAR();
  SCHED();
  STAMP_MID(3)
  rdh(0);
#pragma unroll
  for (int k = 0; k < 16; ++k) {
    if (k == 3) { STAMP_MID(6) BAR(); STAMP_MID(7) }
    SCHED();
    rdh(k + 1);
    if (k >= 1 && k <= 4) {
#pragma unroll
      for (int i = 0; i < 3; ++i) w1f[k - 1][i] = *(const v4u*)(w1x + ((k - 1) * 3 + i) * 1024);
    }
    SCHED();
#pragma unroll
    for (int j = 0; j < 4; ++j)
#pragma unroll
      for (int i = 0; i < 3; ++i) acc[i][j] = mfma(w1f[k % WK][i], hb[k & 1][j], k == 0 ? f32x4{0, 0, 0, 0} : acc[i][j]);
    SCHED();
  }
  float rsd[4];
#pragma unroll
  for (int j = 0; j < 4; ++j) rsd[j] = ((const float*)(shm + L_RSTD))[j * 16 + m];
#pragma unroll
  for (int i = 0; i < 3; ++i)
#pragma unroll
    for (int j = 0; j < 4; ++j) acc[i][j] = mfma(uf[i], hb[0][j], acc[i][j]);
  STAMP_MID(4)
  const unsigned vo = (q & 1) * (NKB_H * 1024) + ((q >> 1) * 16 + m) * 16;
#pragma unroll
  for (int i = 0; i < 3; ++i) {
    if (i < nft) {
      const int ft = ft0 + i;
#pragma unroll
      for (int j = 0; j < 4; j += 2) {
        const f32x4 a = acc[i][j] * rsd[j], bb = acc[i][j + 1] * rsd[j + 1];
        unsigned a0 = pk(fmaxf(a[0], 0.f), fmaxf(a[1], 0.f)), a1v = pk(fmaxf(a[2], 0.f), fmaxf(a[3], 0.f));
        unsigned b0 = pk(fmaxf(bb[0], 0.f), fmaxf(bb[1], 0.f)), b1 = pk(fmaxf(bb[2], 0.f), fmaxf(bb[3], 0.f));
        asm volatile("s_nop 1\n\tv_permlane16_swap_b32 %0, %2\n\tv_permlane16_swap_b32 %1, %3" : "+v"(a0), "+v"(a1v), "+v"(b0), "+v"(b1));
        bst<SC1>(rs, vo, OFF_H1 + ((4 * p + j) * NKB_H + (ft >> 1)) * 1024 + (ft & 1) * 512, v4u{a0, a1v, b0, b1});
      }
    }
  }
  STAMP_MID(12)
  lds_inc(cnt + 2, lane);
  WAIT_V0();
  if (lane == 0) flag_set(ws, rs, FLINE(LN_F3 + p * 4 + (c >> 1)) + ((c & 1) * 4 + w) * 4, tag);
  STAMP_MID(5)
  STAMP_END(w == 0 ? 1 : 5 + w, b, ws)
}

extern "C" void kernel_launch(void* const* d_in, const int* in_sizes, int n_in, void* d_out, int out_size, void* d_ws, size_t ws_size,
                              hipStream_t stream) {
  (void)hipFuncSetAttribute((const void*)fused_all, hipFuncAttributeMaxDynamicSharedMemorySize, K_LDS);
  fused_all<<<256, 512, K_LDS, stream>>>((const char*)d_in[0], (const char*)d_in[1], (const char*)d_in[2], (const char*)d_in[3], (const char*)d_in[4],
                                         (const char*)d_in[6], (const char*)d_in[7], (const char*)d_in[8], (const char*)d_in[9], (char*)d_ws, (char*)d_out);
}
```

```cpp
#include <hip/hip_runtime.h>
#include <hip/hip_bf16.h>
#include <stdint.h>

typedef unsigned v4u __attribute__((ext_vector_type(4)));
typedef unsigned v2u __attribute__((ext_vector_type(2)));
typedef float f32x4 __attribute__((ext_vector_type(4)));
typedef float f32x2 __attribute__((ext_vector_type(2)));
typedef short bf16x8 __attribute__((ext_vector_type(8)));
typedef __bf16 bf2 __attribute__((ext_vector_type(2)));
#define GAS __attribute__((address_space(1)))
#define LAS __attribute__((address_space(3)))
#define WAIT_V(n) asm volatile("s_waitcnt vmcnt(%0)" ::"n"(n) : "memory")
#define CFENCE() asm volatile("" ::: "memory")
#define WAIT_L0() do { CFENCE(); __builtin_amdgcn_s_waitcnt(0xC07F); } while (0)
#define WAIT_V0() do { CFENCE(); __builtin_amdgcn_s_waitcnt(0x0F70); CFENCE(); } while (0)
#define SCHED() __builtin_amdgcn_sched_barrier(0)
#define BAR() do { CFENCE(); __builtin_amdgcn_s_barrier(); CFENCE(); } while (0)
#define RSRC(p, n) __builtin_amdgcn_make_buffer_rsrc((void*)(p), 0, (n), 0x00020000)
constexpr int SC1 = 16, NTP = 2;
#define ABL 0

#define STAMP_BEGIN()
#define STAMP_MID(i) {}
#define STAMP_END(k, blk, ws) {}

constexpr int D = 512, F = 1376;
constexpr int NKB_X = 16, NKB_H = 43;
constexpr unsigned OFF_EPOCH = 0;
#define FSTR 256
constexpr unsigned OFF_FL   = 24u << 20;
constexpr unsigned LN_G1 = 0;
constexpr unsigned LN_G2 = 64;
constexpr unsigned LN_F1 = 128;
constexpr unsigned LN_F3 = 160;
constexpr unsigned LN_F2 = 288;
constexpr unsigned LN_G3 = 416;
constexpr unsigned LN_END = 480;
#define FLINE(l) (OFF_FL + (l) * FSTR)
constexpr unsigned OFF_MOD  = 49152;
constexpr unsigned OFF_RS   = 131072;
constexpr unsigned OFF_ST   = 196608;
constexpr unsigned OFF_XNT  = 524288;
constexpr unsigned OFF_WD   = OFF_XNT + (2u << 20);
constexpr unsigned OFF_W1   = OFF_WD + (1u << 20);
constexpr unsigned OFF_W2   = OFF_W1 + 86 * 16 * 1024;
constexpr unsigned OFF_PQ   = OFF_W2 + 32 * 43 * 1024;
constexpr unsigned OFF_X1   = OFF_PQ + (8u << 20);
constexpr unsigned OFF_H1   = OFF_X1 + (2u << 20);
constexpr unsigned WS_BYTES = FLINE(LN_END);

__device__ __forceinline__ float asf(unsigned u) { return __builtin_bit_cast(float, u); }
__device__ __forceinline__ unsigned asu(float f) { return __builtin_bit_cast(unsigned, f); }
__device__ __forceinline__ float bflo(unsigned u) { return asf(u << 16); }
__device__ __forceinline__ float bfhi(unsigned u) { return asf(u & 0xffff0000u); }
__device__ __forceinline__ unsigned pk(float lo, float hi) {
  f32x2 v = {lo, hi};
  return __builtin_bit_cast(unsigned, __builtin_convertvector(v, bf2));
}
__device__ __forceinline__ float rbf(float x) { return bflo(pk(x, x)); }
__device__ __forceinline__ f32x4 mfma(v4u a, v4u b, f32x4 c) {
  return __builtin_amdgcn_mfma_f32_16x16x32_bf16(__builtin_bit_cast(bf16x8, a), __builtin_bit_cast(bf16x8, b), c, 0, 0, 0);
}
template <int CTRL> __device__ __forceinline__ float dpp_add(float x) {
  int y = __builtin_amdgcn_update_dpp(0, __builtin_bit_cast(int, x), CTRL, 0xf, 0xf, false);
  return x + __builtin_bit_cast(float, y);
}
__device__ __forceinline__ float wave_sum(float x) {
  x = dpp_add<0x111>(x); x = dpp_add<0x112>(x); x = dpp_add<0x114>(x); x = dpp_add<0x118>(x);
  x = dpp_add<0x142>(x); x = dpp_add<0x143>(x);
  return __builtin_bit_cast(float, __builtin_amdgcn_readlane(__builtin_bit_cast(int, x), 63));
}
__device__ __forceinline__ float quad16_sum(float x) {
  float y = x;
  asm volatile("s_nop 1\n\tv_permlane16_swap_b32 %0, %1" : "+v"(x), "+v"(y));
  x += y;
  y = x;
  asm volatile("s_nop 1\n\tv_permlane32_swap_b32 %0, %1" : "+v"(x), "+v"(y));
  return x + y;
}
__device__ __forceinline__ int fresh_lane() { int l; asm volatile("v_mbcnt_lo_u32_b32 %0, -1, 0\n\tv_mbcnt_hi_u32_b32 %0, -1, %0" : "=v"(l)); return l; }
__device__ __forceinline__ v4u ldnt(const void* p) { return __builtin_nontemporal_load((const v4u*)p); }
template <int AUX> __device__ __forceinline__ v4u bld(__amdgpu_buffer_rsrc_t rs, unsigned voff, unsigned soff) { return __builtin_amdgcn_raw_buffer_load_b128(rs, voff, soff, AUX); }
template <int AUX> __device__ __forceinline__ void bst(__amdgpu_buffer_rsrc_t rs, unsigned voff, unsigned soff, v4u v) { __builtin_amdgcn_raw_buffer_store_b128(v, rs, voff + soff, 0, AUX); }
__device__ __forceinline__ void st1(__amdgpu_buffer_rsrc_t rs, unsigned voff, unsigned soff, unsigned v) { __builtin_amdgcn_raw_buffer_store_b32(v, rs, voff, soff, SC1); }
#define FLAG_MODE 1
__device__ __forceinline__ void flag_set(char* ws, __amdgpu_buffer_rsrc_t rs, unsigned off, unsigned v) {
  asm volatile("global_atomic_swap %0, %1, %2 sc1" :: "v"(off), "v"(v), "s"(ws) : "memory");
}
__device__ __forceinline__ unsigned ld1(__amdgpu_buffer_rsrc_t rs, unsigned voff, unsigned soff) { return __builtin_amdgcn_raw_buffer_load_b32(rs, voff, soff, SC1); }
template <int AUX> __device__ __forceinline__ void glds16(const char* g, LAS char* l) {
  __builtin_amdgcn_global_load_lds((const GAS unsigned*)g, (LAS unsigned*)l, 16, 0, AUX);
}

#define AUX_OUT 0
#define JOB_LATE 1
#define W_PRE 4
#define WD_C 12
#define W1_B1 11
#define W1_TRIG 13
#define KW 0
#define AUXW0 2
#define AUXW1 2
#define NREP 8
#define G1_SLEEP 2
#define W_EARLY 1
#define W_HOLD 0
#define JOBPERM 1
#define LATE_ST 1
#define LATE_SLEEP 64
#define AUX_X 16
#define WD_A 0
#define WD_B 8
#define W1_NB1 8
#define W1_AH 2
#define POLL_SLEEP 2
constexpr int NS = 4, STAGE = 32768;
constexpr int L_TOP = NS * STAGE;
constexpr int L_RAW = L_TOP;
constexpr int L_MB = L_RAW + 14336;
constexpr int L_SL = L_MB + 3072;
constexpr int L_ZERO = L_TOP + 24576;
constexpr int L_CNT = L_ZERO + 64;
constexpr int L_RSTD = L_CNT + 64;
constexpr int K_LDS = L_RSTD + 256;
constexpr int L_LN = 3 * STAGE;
constexpr int L_H2 = 0, H2W = 17 * 1024;
constexpr int L_W1X = 4 * H2W;
constexpr unsigned SPIN_CAP = 1u << 16;

template <int SLP = POLL_SLEEP>
__device__ __forceinline__ void wait_words(__amdgpu_buffer_rsrc_t rs, unsigned voff, unsigned soff, unsigned tag, bool sel) {
#pragma unroll 1
  for (unsigned spins = 0; spins < SPIN_CAP; ++spins) {
    CFENCE();
    const unsigned v = ld1(rs, voff, soff);
    if (__builtin_amdgcn_ballot_w64(sel && v != tag) == 0ull) break;
    __builtin_amdgcn_s_sleep(SLP);
  }
  __builtin_amdgcn_fence(__ATOMIC_ACQUIRE, "wavefront");
  CFENCE();
  SCHED();
}
__device__ __forceinline__ void wait_lds(volatile LAS unsigned* p, unsigned n) {
#pragma unroll 1
  for (unsigned spins = 0; spins < SPIN_CAP * 64; ++spins) {
    if (__builtin_amdgcn_readfirstlane(*p) >= n) break;
    __builtin_amdgcn_s_sleep(1);
  }
  CFENCE();
  SCHED();
}
__device__ __forceinline__ void wait_lds2(volatile LAS unsigned* p, unsigned n) {
  const unsigned a = (unsigned)(uintptr_t)p;
#pragma unroll 1
  for (unsigned spins = 0; spins < SPIN_CAP * 64; ++spins) {
    unsigned v;
    asm volatile("ds_read_b32 %0, %1\n\ts_waitcnt lgkmcnt(0)" : "=v"(v) : "v"(a) : "memory");
    if (__builtin_amdgcn_readfirstlane(v) >= n) break;
    __builtin_amdgcn_s_sleep(1);
  }
  SCHED();
}
__device__ __forceinline__ unsigned lds_inc(volatile LAS unsigned* p, int lane) {
  unsigned old = 0;
  if (lane == 0) old = __hip_atomic_fetch_add((LAS unsigned*)p, 1u, __ATOMIC_RELAXED, __HIP_MEMORY_SCOPE_WORKGROUP);
  return __builtin_amdgcn_readfirstlane(old);
}

__global__ __launch_bounds__(512, 2) void fused_all(const char* __restrict__ x, const char* __restrict__ wsr, const char* __restrict__ wsi,
                                                    const char* __restrict__ wdr, const char* __restrict__ wdi,
                                                    const char* __restrict__ w1, const char* __restrict__ w2, const char* __restrict__ cvec,
                                                    const char* __restrict__ wmod, char* __restrict__ ws, char* __restrict__ out) {
  extern __shared__ __attribute__((aligned(16))) char shm[];
  STAMP_BEGIN()
  const int b = blockIdx.x, xcd = b & 7, idx = b >> 3, pl = idx >> 3, p = xcd * 4 + pl, c = idx & 7, dq = c & 3, kh = c >> 2;
  const int w8 = __builtin_amdgcn_readfirstlane(threadIdx.x >> 6), w = w8 & 3;
  const __amdgpu_buffer_rsrc_t rs = RSRC(ws, WS_BYTES);
  const float* sll = (const float*)(shm + L_SL);
  volatile LAS unsigned* cnt = (volatile LAS unsigned*)((LAS char*)shm + L_CNT);

  v4u xq; unsigned tq;
  asm volatile("global_load_dwordx4 %0, %2, %3 nt\n\tglobal_load_dword %1, %4, %5 sc1"
               : "=&v"(xq), "=&v"(tq) : "v"(b * 8192 + threadIdx.x * 16), "s"(x), "v"(OFF_EPOCH), "s"(ws) : "memory");
  SCHED();
  v4u R[8], cvv;
  const int jb = c * 32 + pl * 8 + xcd;
  const int jm = ((c * 2 + (pl >> 1)) * 8 + xcd) * 2 + (pl & 1);
  const bool is1 = w == 0;
  const int jk0 = (is1 ? (jb & 7) : (jb >> 3)) * 64, jn0 = (is1 ? (jb >> 3) : (jb & 7)) * 64;
  {
    const int lane = threadIdx.x & 63, m = lane & 15, q = lane >> 4, kg = lane >> 3, ng = lane & 7;
    const char* base; unsigned off, ld;
    if (w8 >= 4 || (w >= 2 ? jm >= 192 : jb >= 240)) { base = cvec; ld = 0; off = 0; }
    else if (w >= 2) { base = wmod; ld = 6144; off = ((jm / 96) * 256 + (w & 1) * 128 + (lane >> 2) * 8) * 6144 + (jm % 96) * 64 + (lane & 3) * 16; }
    else if (jb < 176) {
      base = is1 ? w1 : w2; ld = is1 ? F * 2 : D * 2;
      const bool ok = (jk0 + kg * 8 < (is1 ? D : F)) && (jn0 + ng * 8 < (is1 ? F : D));
      off = ok ? (jk0 + kg * 8) * ld + (jn0 + ng * 8) * 2 : 0;
    } else {
      const int j3 = w * 64 + jb - 176, et = j3 >> 2, kb0 = (j3 & 3) * 8;
      base = kb0 < 16 ? wdr : wdi; ld = 64;
      off = (32 * (et >> 1) + 8 * (m >> 2) + 4 * (et & 1) + (m & 3)) * 1024 + (kb0 & 15) * 64 + q * 16;
    }
    ld = __builtin_amdgcn_readfirstlane(ld);
    const __amdgpu_buffer_rsrc_t rj = RSRC(base, 0x7fffffff);
#pragma unroll
    for (int k = 0; k < 8; ++k) R[k] = bld<NTP>(rj, off, k * ld);
    cvv = bld<0>(RSRC(cvec, 1024), ((w & 1) * 128 + (lane >> 2) * 8) * 2, (jm / 96 & 1) * 512);
    SCHED();
  }
  if (w8 < 4 || W_EARLY == 0) {
    WAIT_V(9);
  } else {
    const int lane = threadIdx.x & 63;
    if (W_EARLY > 0) {
      const int gq = (w + dq + 1) & 3;
      const char* src = (gq < 2 ? wsr : wsi) + (size_t)(p * 64 + (gq & 1) * 32 + (lane >> 3)) * 4096 + kh * 2048;
      const int sl = lane & 7, h = lane >> 4;
#pragma unroll
      for (int st = 0; st < W_EARLY; ++st)
#pragma unroll
        for (int i = 0; i < 4; ++i)
          glds16<NTP>(src + ((sl ^ (4 * (i & 1) + h)) * 16) + i * 8 * 4096 + st * 128, (LAS char*)shm + st * STAGE + w * 4096 + 16384 + i * 1024);
    }
    SCHED();
    WAIT_V(9 + 4 * W_EARLY);
  }
  asm volatile("" : "+v"(xq), "+v"(tq) :: "memory");
  unsigned tag;
  {
    const int tid = threadIdx.x, lane = tid & 63;
    float v[8];
#pragma unroll
    for (int u = 0; u < 4; ++u) { v[2 * u] = bflo(xq[u]); v[2 * u + 1] = bfhi(xq[u]); }
    const float mean = wave_sum(((v[0] + v[1]) + (v[2] + v[3])) + ((v[4] + v[5]) + (v[6] + v[7]))) * (1.f / D);
    float ss = 0.f;
#pragma unroll
    for (int j = 0; j < 8; ++j) { const float t = v[j] - mean; ss += t * t; }
    const float var = wave_sum(ss) * (1.f / D);
    const float mb = rbf(mean);
    const float rsd = 1.f / rbf(__builtin_sqrtf(rbf(rbf(var) + 1e-5f)));
    v4u o;
#pragma unroll
    for (int u = 0; u < 4; ++u) {
      const unsigned d2 = pk(v[2 * u] - mb, v[2 * u + 1] - mb);
      o[u] = pk(bflo(d2) * rsd, bfhi(d2) * rsd);
    }
    *(v4u*)(shm + L_LN + tid * 16) = o;
    if (tid < 32) ((unsigned*)(shm + L_ZERO))[tid] = 0u;
    WAIT_L0();
    BAR();
    const int d = (tid & ~31) + 8 * ((tid >> 2) & 3) + 4 * ((tid >> 4) & 1) + (tid & 3);
    const unsigned short* sp = (const unsigned short*)(shm + L_LN) + d;
    v4u t;
#pragma unroll
    for (int u = 0; u < 4; ++u) t[u] = (unsigned)sp[(2 * u) * 512] | ((unsigned)sp[(2 * u + 1) * 512] << 16);
    tag = __builtin_amdgcn_readfirstlane(tq) + 1u;
    bst<SC1>(rs, tid * 16, OFF_XNT + b * 8192, t);
    WAIT_V0();
    BAR();
    if (w8 == 0) { if (lane < NREP) flag_set(ws, rs, FLINE(LN_G1 + lane * 8 + (b >> 5)) + (b & 31) * 4, tag); }
    else __builtin_amdgcn_s_sleep(1);
  }
  if (w8 >= 4) {
    const int lane = fresh_lane(), m = lane & 15, q = lane >> 4;
    const unsigned l16 = lane * 16;
    v4u wd[8][4], res[2];
    {
      const int gq = (w + dq + 1) & 3;
      const char* wbase = (gq < 2 ? wsr : wsi) + (size_t)(p * 64 + (gq & 1) * 32 + (lane >> 3)) * 4096 + kh * 2048;
      const int sl = lane & 7, h = lane >> 4;
      const char* wsrc0 = wbase + ((sl ^ h) * 16);
      const char* wsrc1 = wbase + ((sl ^ (4 + h)) * 16) + 8 * 4096;
      const char* xsrc = ws + OFF_XNT + ((size_t)(kh * 128 + 2 * w) * 512 + dq * 128 + lane) * 16;
      auto issW0 = [&](int st) {
        LAS char* dst = (LAS char*)shm + (st & 3) * STAGE + w * 4096 + 16384;
        const int kk = (st + KW * dq) & 15;
#pragma unroll
        for (int i = 0; i < 4; ++i) glds16<AUXW0>(((i & 1) ? wsrc1 : wsrc0) + (size_t)(i >> 1) * 16 * 4096 + kk * 128, dst + i * 1024);
      };
      auto issW = [&](int st) {
        LAS char* dst = (LAS char*)shm + (st & 3) * STAGE + w * 4096 + 16384;
        const int kk = (st + KW * dq) & 15;
#pragma unroll
        for (int i = 0; i < 4; ++i) glds16<AUXW1>(((i & 1) ? wsrc1 : wsrc0) + (size_t)(i >> 1) * 16 * 4096 + kk * 128, dst + i * 1024);
      };
      auto issX = [&](int st) {
        LAS char* dst = (LAS char*)shm + (st & 3) * STAGE + w * 4096;
#pragma unroll
        for (int i = 0; i < 4; ++i) glds16<AUX_X>(xsrc + (size_t)((st + KW * dq) & 15) * 65536 + (i >> 1) * 8192 + (i & 1) * 1024, dst + i * 1024);
      };
      STAMP_MID(13)
      if (W_HOLD) wait_lds2(cnt + 12, 4);
#pragma unroll
      for (int i = W_EARLY; i < W_PRE; ++i) issW0(i);
#pragma unroll
      for (int ip = 0; ip < 2; ++ip) res[ip] = bld<NTP>(RSRC(x, 2048 * 1024), m * 1024 + q * 16 + ip * 64, (p * 64 + w * 16) * 1024 + c * 128);
      SCHED();
      if (w < 2) { wait_words<G1_SLEEP>(rs, (lane >> 5) * FSTR + (lane & 31) * 4, FLINE(LN_G1 + (xcd % NREP) * 8 + kh * 4 + w * 2), tag, true); lds_inc(cnt + 1, lane); }
      wait_lds(cnt + 1, 2);
      STAMP_MID(14)
      issX(0); issX(1);
#pragma unroll
      for (int i = W_PRE; i < 4; ++i) issW0(i);
      issX(2); issX(3);
      WAIT_V(12 + 4 * (4 - W_PRE)); BAR();
      WAIT_V(8 + 4 * (4 - W_PRE)); BAR();
      issX(4);
      WAIT_V(8); BAR();
      issW(4); issW(5);
      WAIT_V(12); BAR();
      issX(5); issX(6);
#pragma unroll 1
      for (int t = 3; t < 11; t += 2) {
        WAIT_V(12); BAR();
        issW(t + 3); issW(t + 4);
        WAIT_V(12); BAR();
        issX(t + 4); issX(t + 5);
      }
      WAIT_V(12); BAR();
      issW(14); issW(15);
      STAMP_MID(0)
      const unsigned a0 = OFF_WD + ((4 * c) * 32 + 8 * w) * 1024;
      auto ldwd = [&](int f0, int f1) {
        SCHED();
#pragma unroll
        for (int f = f0; f < f1; ++f) wd[f >> 2][f & 3] = bld<SC1>(rs, l16, a0 + ((f & 3) * 32 + (f >> 2)) * 1024);
        SCHED();
      };
      WAIT_V(12); BAR();
      issX(15);
      {
        const char* mp = ws + OFF_MOD + (w & 1) * 12288;
        LAS char* dst = (LAS char*)shm + L_RAW + (w & 1) * 7168;
        const unsigned sl_off = lane < 32 ? 2048 + dq * 512 + l16 : (lane < 48 ? 4096 + c * 256 + l16 - 512 : 10240 + c * 256 + l16 - 768);
#pragma unroll
        for (int i = 0; i < 4; ++i) {
          const int pc = (w >> 1) * 4 + i;
          const unsigned so = pc < 2 ? pc * 1024 + l16 : (pc < 6 ? 4096 + pc * 1024 + l16 : sl_off);
          glds16<SC1>(mp + so, dst + (pc < 6 ? pc : 6) * 1024);
        }
      }
      ldwd(0, WD_A);
      WAIT_V(8 + 4 + WD_A); BAR();
      ldwd(WD_A, WD_A + WD_B);
      WAIT_V(WD_A + WD_B); BAR();
      ldwd(WD_A + WD_B, WD_A + WD_B + WD_C);
      {
        const int tid = w * 64 + lane;
        f32x2 pa[3], pb[3];
        float sa1, sb1;
        asm volatile("ds_read_b64 %0, %8\n\tds_read_b64 %1, %8 offset:2048\n\tds_read_b64 %2, %8 offset:4096\n\t"
                     "ds_read_b64 %3, %8 offset:7168\n\tds_read_b64 %4, %8 offset:9216\n\tds_read_b64 %5, %8 offset:11264\n\t"
                     "ds_read_b32 %6, %9\n\tds_read_b32 %7, %9 offset:7168\n\ts_waitcnt lgkmcnt(0)"
                     : "=&v"(pa[0]), "=&v"(pa[1]), "=&v"(pa[2]), "=&v"(pb[0]), "=&v"(pb[1]), "=&v"(pb[2]), "=&v"(sa1), "=&v"(sb1)
                     : "v"(L_RAW + tid * 8), "v"(tid < 128 ? L_RAW + 6656 + tid * 4 : L_RAW + 4096 + c * 256 + (tid & 63) * 4) : "memory");
        unsigned z[3];
#pragma unroll
        for (int i = 0; i < 3; ++i) {
          z[i] = pk(pa[i][0] + pb[i][0], pa[i][1] + pb[i][1]);
          if (i == 2) z[i] = pk(bflo(z[i]) + 1.f, bfhi(z[i]) + 1.f);
        }
        float sv = rbf(sa1 + sb1);
        if (tid >= 128) sv = rbf(sv + 1.f);
        asm volatile("ds_write_b32 %0, %1\n\tds_write_b32 %0, %2 offset:1024\n\tds_write_b32 %0, %3 offset:2048\n\tds_write_b32 %0, %4 offset:3072\n\ts_waitcnt lgkmcnt(0)"
                     :: "v"(L_MB + tid * 4), "v"(z[0]), "v"(z[1]), "v"(z[2]), "v"(sv) : "memory");
        asm volatile("ds_add_u32 %0, %1" :: "v"(L_CNT + 13 * 4), "v"(lane == 0 ? 1u : 0u) : "memory");
      }
      if (WD_A + WD_B + WD_C < 32) { wait_lds2(cnt + 6, 4); ldwd(WD_A + WD_B + WD_C, 32); }
      wait_lds2(cnt + 13, 4);
    }
    SCHED();
    f32x4 acc[4][4];
    f32x4* red = (f32x4*)(shm + L_H2);
    auto put = [&](int j, int ln) {
#pragma unroll
      for (int i = 0; i < 4; ++i) red[((j + w) & 3) * (H2W / 16) + ((j - 1) * 4 + i) * 64 + ln] = acc[i][j];
    };
    {
      f32x4 accS[4];
      v4u sf[8];
#pragma unroll
      for (int k = 0; k < 8; ++k) sf[k] = *(const v4u*)(shm + L_MB + ((8 * (w & 1) + k) * 32 + q * 8) * 2);
      SCHED();
#pragma unroll
      for (int k = 0; k < 8; ++k)
#pragma unroll
        for (int i = 0; i < 4; ++i) accS[i] = mfma(wd[k][i], sf[k], k == 0 ? f32x4{0, 0, 0, 0} : accS[i]);
      SCHED();
      STAMP_MID(1)
      if (w == 0) { wait_words(rs, (lane & 31) * 4, FLINE(LN_F1 + p), tag, true); lds_inc(cnt + 4, lane); }
      else wait_lds(cnt + 4, 1);
      STAMP_MID(2)
      f32x2 rr[4];
#pragma unroll
      for (int j = 0; j < 4; ++j)
#pragma unroll
        for (int h = 0; h < 2; ++h) rr[j][h] = asf(ld1(rs, m * 4 + h * 128, OFF_RS + (p * 2 + (w >> 1)) * 1024 + ((j + w) & 3) * 256));
      constexpr int WIN = 2;
      v4u pq[WIN][4];
      unsigned b0[4];
#pragma unroll
      for (int j = 0; j < 4; ++j) b0[j] = OFF_PQ + ((4 * p + ((j + w) & 3)) * 64 + 16 * w) * 1024;
#pragma unroll
      for (int g = 0; g < WIN; ++g)
#pragma unroll
        for (int jj = 0; jj < 4; ++jj) { const int j = 3 - jj; pq[g][j] = bld<SC1>(rs, l16, b0[j] + g * 1024); }
      SCHED();
#pragma unroll
      for (int g = 0; g < 16; ++g) {
#pragma unroll
        for (int jj = 0; jj < 4; ++jj) {
          const int j = 3 - jj;
          const v4u cur = pq[g % WIN][j];
#pragma unroll
          for (int i = 0; i < 4; ++i) acc[i][j] = mfma(wd[g >> 1][i], cur, g == 0 ? accS[i] * (rr[j][0] + rr[j][1]) : acc[i][j]);
          if (g + WIN < 16) pq[g % WIN][j] = bld<SC1>(rs, l16, b0[j] + (g + WIN) * 1024);
          if (g == 15 && j < 3) put(j + 1, lane);
          if (g == W1_TRIG && j == 0) lds_inc(cnt + 7, lane);
          SCHED();
        }
      }
    }
    STAMP_MID(3)
    auto reduce_gate = [&](const float* g, int ln, int ci) {
      const int q = ln >> 4;
      f32x4 gv[4];
#pragma unroll
      for (int i = 0; i < 4; ++i) gv[i] = *(const f32x4*)(g + 32 * (i >> 1) + 8 * q + 4 * (i & 1));
      WAIT_L0();
      lds_inc(cnt + ci, ln);
      wait_lds(cnt + ci, 4);
      f32x4 pt[3][4];
#pragma unroll
      for (int i = 0; i < 4; ++i)
#pragma unroll
        for (int s = 0; s < 3; ++s) pt[s][i] = red[w * (H2W / 16) + (s * 4 + i) * 64 + ln];
      SCHED();
#pragma unroll
      for (int i = 0; i < 4; ++i) {
        const f32x4 t = ((acc[i][0] + pt[0][i]) + (pt[1][i] + pt[2][i])) * gv[i];
        const unsigned r0 = res[i >> 1][2 * (i & 1)], r1 = res[i >> 1][2 * (i & 1) + 1];
        res[i >> 1][2 * (i & 1)] = pk(bflo(r0) + t[0], bfhi(r0) + t[1]);
        res[i >> 1][2 * (i & 1) + 1] = pk(bflo(r1) + t[2], bfhi(r1) + t[3]);
      }
    };
    const unsigned xb = OFF_X1 + (4 * p + w) * NKB_X * 1024, sb0 = OFF_ST + (p * 4 + w) * 1024;
    {
      f32x4 cv[4];
#pragma unroll
      for (int i = 0; i < 4; ++i) cv[i] = *(const f32x4*)(sll + 128 + 32 * (i >> 1) + 8 * q + 4 * (i & 1));
      reduce_gate(sll, lane, 10);
      STAMP_MID(10)
      float v[16];
#pragma unroll
      for (int u = 0; u < 8; ++u) { v[2 * u] = bflo(res[u >> 2][u & 3]); v[2 * u + 1] = bfhi(res[u >> 2][u & 3]); }
      float s = 0.f;
#pragma unroll
      for (int u = 0; u < 16; u += 4) s += (v[u] + v[u + 1]) + (v[u + 2] + v[u + 3]);
      const float mc = quad16_sum(s) * (1.f / 64);
#pragma unroll
      for (int u = 0; u < 16; ++u) v[u] -= mc;
#pragma unroll
      for (int ip = 0; ip < 2; ++ip) {
        v4u o;
#pragma unroll
        for (int u = 0; u < 4; ++u) o[u] = pk(v[8 * ip + 2 * u] * cv[2 * ip + (u >> 1)][(2 * u) & 3], v[8 * ip + 2 * u + 1] * cv[2 * ip + (u >> 1)][(2 * u + 1) & 3]);
        bst<SC1>(rs, l16, xb + (2 * c + ip) * 1024, o);
        *(v4u*)(shm + L_H2 + w * H2W + ip * 1024 + l16) = o;
      }
      float ss = 0.f, s2 = 0.f;
#pragma unroll
      for (int u = 0; u < 16; u += 2) { ss += v[u] * v[u]; s2 += v[u + 1] * v[u + 1]; }
      ss = quad16_sum(ss + s2);
      if (q == 0) __builtin_amdgcn_raw_buffer_store_b64(v2u{asu(mc), asu(ss)}, rs, m * 8, sb0 + c * 128, SC1);
      STAMP_MID(11)
      WAIT_V0();
      if (lane == 0) flag_set(ws, rs, FLINE(LN_F2 + p * 4 + w) + c * 4, tag);
    }
    asm volatile("" : "+v"(res[0]), "+v"(res[1]));
    SCHED();
    STAMP_MID(4)
    wait_words(rs, (lane & 7) * 4, FLINE(LN_F2 + p * 4 + w), tag, true);
    STAMP_MID(5)
    {
      v4u X[14];
      v2u st[8];
#pragma unroll
      for (int k = 0; k < 14; ++k) X[k] = bld<SC1>(rs, l16, xb + ((2 * c + 2 + k) & 15) * 1024);
#pragma unroll
      for (int i = 0; i < 8; ++i) st[i] = __builtin_amdgcn_raw_buffer_load_b64(rs, m * 8 + i * 128, sb0, SC1);
      SCHED();
      lds_inc(cnt + 8, lane);
      SCHED();
#pragma unroll
      for (int k = 0; k < 14; ++k) {
        *(v4u*)(shm + L_H2 + w * H2W + (2 + k) * 1024 + l16) = X[k];
        if (k == 1) { WAIT_L0(); BAR(); SCHED(); }
      }
      float mu[8], mean = 0.f, m2 = 0.f;
#pragma unroll
      for (int i = 0; i < 8; ++i) { mu[i] = asf(st[i].x); mean += mu[i]; m2 += asf(st[i].y); }
      mean *= 0.125f;
      float dv = 0.f;
#pragma unroll
      for (int i = 0; i < 8; ++i) { const float d0 = mu[i] - mean; dv += d0 * d0; }
      m2 += 64.f * dv;
      const float den = rbf(__builtin_sqrtf(rbf(rbf(m2 * (1.f / D)) + 1e-5f)));
      const float mb = rbf(mean);
      unsigned d0 = q == 0 ? pk(mu[0] - mb, mu[1] - mb) : pk(mu[4] - mb, mu[5] - mb);
      unsigned d1 = q == 0 ? pk(mu[2] - mb, mu[3] - mb) : pk(mu[6] - mb, mu[7] - mb);
      if (q >= 2) { d0 = q == 2 ? pk(den, 0.f) : 0u; d1 = 0u; }
      *(v4u*)(shm + L_H2 + w * H2W + 16 * 1024 + l16) = v4u{d0, d1, d0, d1};
      if (q == 0) ((float*)(shm + L_RSTD))[w * 16 + m] = 1.f / den;
      WAIT_L0(); BAR(); SCHED();
    }
    STAMP_MID(6)
    {
      constexpr int WW = 8, WIN = 2;
      const int nk = w == 3 ? 10 : 11;
      v4u w2f[WW][4];
      const unsigned a0 = OFF_W2 + ((4 * c) * NKB_H + 11 * w) * 1024;
      wait_lds(cnt + 9, 4);
#pragma unroll
      for (int k = 0; k < WW; ++k)
#pragma unroll
        for (int i = 0; i < 4; ++i) w2f[k][i] = bld<SC1>(rs, l16, a0 + (i * NKB_H + k) * 1024);
      SCHED();
      wait_lds(cnt + 2, 4);
      { const int ln = fresh_lane(); wait_words(rs, (ln & 7) * 4, FLINE(LN_F3 + p * 4 + w), tag, true); }
      STAMP_MID(7)
      v4u hf[WIN][4];
      unsigned b0[4];
#pragma unroll
      for (int j = 0; j < 4; ++j) b0[j] = OFF_H1 + ((4 * p + ((j + w) & 3)) * NKB_H + 11 * w) * 1024;
      const unsigned last = w == 3 ? 9 * 1024 : 10 * 1024;
      auto ldh = [&](int g, int j) { hf[g % WIN][j] = bld<SC1>(rs, l16, b0[j] + (g < 10 ? g * 1024 : last)); };
#pragma unroll
      for (int g = 0; g < WIN; ++g)
#pragma unroll
        for (int jj = 0; jj < 4; ++jj) ldh(g, 3 - jj);
      SCHED();
#pragma unroll
      for (int g = 0; g < 11; ++g) {
        if (g < 10 || nk == 11) {
#pragma unroll
          for (int jj = 0; jj < 4; ++jj) {
            const int j = 3 - jj;
            const v4u cur = hf[g % WIN][j];
#pragma unroll
            for (int i = 0; i < 4; ++i) acc[i][j] = mfma(w2f[g % WW][i], cur, g == 0 ? f32x4{0, 0, 0, 0} : acc[i][j]);
            if (g + WIN < 11) ldh(g + WIN, j);
            SCHED();
          }
          if (g + WW < 11) {
#pragma unroll
            for (int i = 0; i < 4; ++i) w2f[g % WW][i] = bld<SC1>(rs, l16, a0 + i * NKB_H * 1024 + (g + WW < 10 ? (g + WW) * 1024 : last));
            SCHED();
          }
        }
      }
      STAMP_MID(12)
      const int ln = fresh_lane();
      put(3, ln); put(2, ln); put(1, ln);
      reduce_gate(sll + 64, ln, 11);
      STAMP_MID(15)
#pragma unroll
      for (int ip = 0; ip < 2; ++ip) bst<AUX_OUT>(RSRC(out, 2048 * 1024), (ln & 15) * 1024 + (ln >> 4) * 16 + ip * 64, (p * 64 + w * 16) * 1024 + c * 128, res[ip]);
      if (b == 0 && w == 0 && ln == 0) st1(rs, 0, OFF_EPOCH, tag);
    }
    STAMP_END(w == 0 ? 2 : 8 + w, b, ws)
    return;
  }

  const int lane = fresh_lane(), m = lane & 15, q = lane >> 4, tid = w * 64 + lane;
  const unsigned l16 = lane * 16;
  const int wr = w >> 1, wc = w & 1;
  const int kg = lane >> 3, ng = lane & 7;
  auto job_store = [&]() {
    const int nkb = is1 ? NKB_X : NKB_H;
    const unsigned dst = (is1 ? OFF_W1 : OFF_W2) + ((jn0 >> 4) * nkb + (jk0 >> 5)) * 1024;
#pragma unroll
    for (int f = 0; f < 8; ++f) {
      const int ntl = f >> 1, kbl = f & 1;
      if (jk0 + kbl * 32 < (is1 ? D : F) && jn0 + (ntl >> 1) * 32 < (is1 ? F : D)) bst<SC1>(rs, l16, dst + (ntl * nkb + kbl) * 1024, R[f]);
    }
  };
  STAMP_MID(13)
  if (w >= 2) {
    if (jm < 192) {
      float a8[8] = {0.f, 0.f, 0.f, 0.f, 0.f, 0.f, 0.f, 0.f};
#pragma unroll
      for (int i = 0; i < 8; ++i) {
        const float c0 = (i & 1) ? bfhi(cvv[i >> 1]) : bflo(cvv[i >> 1]);
        const float sl = rbf(c0 / (1.f + __expf(-c0)));
#pragma unroll
        for (int u = 0; u < 4; ++u) { a8[2 * u] += sl * bflo(R[i][u]); a8[2 * u + 1] += sl * bfhi(R[i][u]); }
      }
      float* red = (float*)(shm + L_TOP + 16384);
      *(f32x4*)(red + ((w & 1) * 16 + (lane >> 2)) * 36 + (lane & 3) * 8) = f32x4{a8[0], a8[1], a8[2], a8[3]};
      *(f32x4*)(red + ((w & 1) * 16 + (lane >> 2)) * 36 + (lane & 3) * 8 + 4) = f32x4{a8[4], a8[5], a8[6], a8[7]};
      WAIT_L0();
      if (lds_inc(cnt + 5, lane) == 1u) {
        CFENCE();
        float s = 0.f;
#pragma unroll
        for (int i = 0; i < 16; ++i) s += red[((lane >> 5) * 16 + i) * 36 + (lane & 31)];
        float s2 = s;
        asm volatile("s_nop 1\n\tv_permlane32_swap_b32 %0, %1" : "+v"(s), "+v"(s2));
        s += s2;
        if (lane < 32) st1(rs, lane * 4, OFF_MOD + ((jm / 96) * 3072 + (jm % 96) * 32) * 4, asu(s));
      }
    }
  } else if (jb < 176) {
    char* lds = shm + L_TOP + w * 8192;
    const int a = ng & 3, qq = kg & 3;
    char* wp = lds + ((ng >> 2) * 4 + (kg >> 2)) * 1024 + (is1 ? (a >> 1) * 2048 + (qq * 16 + 8 * (a & 1)) * 16 : (qq * 16 + 4 * a) * 16);
    const int hi = is1 ? 64 : 2048;
#pragma unroll
    for (int jn = 0; jn < 8; ++jn) {
      const unsigned sel = (jn & 1) ? 0x07060302u : 0x05040100u;
      const int u = jn >> 1;
      v4u o;
      o.x = __builtin_amdgcn_perm(R[1][u], R[0][u], sel);
      o.y = __builtin_amdgcn_perm(R[3][u], R[2][u], sel);
      o.z = __builtin_amdgcn_perm(R[5][u], R[4][u], sel);
      o.w = __builtin_amdgcn_perm(R[7][u], R[6][u], sel);
      *(v4u*)(wp + (jn >> 2) * hi + (jn & 3) * 16) = o;
    }
    WAIT_L0();
    CFENCE();
#pragma unroll
    for (int f = 0; f < 8; ++f) R[f] = *(const v4u*)(lds + f * 1024 + l16);
    if (!LATE_ST) job_store();
  } else if (jb < 240) {
#pragma unroll
    for (int i = 0; i < 8; ++i) bst<SC1>(rs, l16, OFF_WD + ((w * 64 + jb - 176) * 8 + i) * 1024, R[i]);
  }
  SCHED();
  STAMP_MID(14)
  lds_inc(cnt + 12, lane);
  const int ft0 = (c < 6 ? 11 * c : 66 + 10 * (c - 6)) + 3 * w;
  const int nft = w < 3 ? 3 : (c < 6 ? 2 : 1);
  {
    const int xoff = q * 2048 + (wc * 64 + m) * 16;
    int wo[2];
#pragma unroll
    for (int kk = 0; kk < 2; ++kk) wo[kk] = 16384 + wr * 8192 + m * 128 + (((4 * kk + q) ^ ((m >> 1) & 7)) * 16);
    f32x4 acc[4][4], rsum = {0, 0, 0, 0};
#pragma unroll
    for (int i = 0; i < 4; ++i)
#pragma unroll
      for (int j = 0; j < 4; ++j) acc[i][j] = f32x4{0, 0, 0, 0};
    const v4u ones = {0x3f803f80u, 0x3f803f80u, 0x3f803f80u, 0x3f803f80u};
    v4u XA[4], WA[4], XB[4], WB[4];
    int sA = 0;
    auto rd = [&](int kk, v4u (&X)[4], v4u (&W)[4]) {
      W[0] = *(const v4u*)(shm + sA + wo[kk]);
      W[1] = *(const v4u*)(shm + sA + wo[kk] + 2048);
      W[2] = *(const v4u*)(shm + sA + wo[kk] + 6144 - wc * 2048);
      W[3] = *(const v4u*)(shm + sA + wo[kk] + 4096 + wc * 2048);
#pragma unroll
      for (int j = 0; j < 4; ++j) X[j] = *(const v4u*)(shm + sA + xoff + kk * 8192 + j * 256);
      if (kk) sA = (sA + STAGE) & (NS * STAGE - 1);
    };
    auto mm = [&](v4u (&X)[4], v4u (&W)[4]) {
#pragma unroll
      for (int j = 0; j < 4; ++j)
#pragma unroll
        for (int i = 0; i < 4; ++i) acc[i][j] = mfma(X[i], W[j], acc[i][j]);
      rsum = mfma(ones, W[3], rsum);
    };
    auto mix = [&]() {
#pragma unroll
      for (int i = 0; i < 8; ++i) {
        __builtin_amdgcn_sched_group_barrier(0x008, 1, 0);
        __builtin_amdgcn_sched_group_barrier(0x100, 1, 0);
      }
      __builtin_amdgcn_sched_group_barrier(0x008, 9, 0);
    };
    auto steps = [&](int n) {
#pragma unroll 1
      for (int t = 0; t < n; ++t) {
        SCHED();
        rd(1, XB, WB); mm(XA, WA); mix();
        SCHED();
        WAIT_L0();
        BAR();
        SCHED();
        rd(0, XA, WA); mm(XB, WB); mix();
      }
      SCHED();
    };
    BAR();
    STAMP_MID(0)
    rd(0, XA, WA);
    steps(2);
    WAIT_V0();
    if (lds_inc(cnt + 3, lane) == 3u) { if (lane < 8) flag_set(ws, rs, FLINE(LN_G2 + lane * 8 + (b >> 5)) + (b & 31) * 4, tag); }
    steps(7);
    STAMP_MID(8)
    unsigned g2 = ld1(rs, (lane >> 5) * FSTR + (lane & 31) * 4, FLINE(LN_G2 + xcd * 8 + w * 2));
    steps(2);
    if (__builtin_amdgcn_ballot_w64(g2 != tag) != 0ull) wait_words(rs, (lane >> 5) * FSTR + (lane & 31) * 4, FLINE(LN_G2 + xcd * 8 + w * 2), tag, true);
    CFENCE();
    STAMP_MID(9)
    steps(4);
    rd(1, XB, WB); mm(XA, WA); mix();
    SCHED();
    mm(XB, WB);
    STAMP_MID(1)
    f32x4 cav[4];
#pragma unroll
    for (int i = 0; i < 4; ++i) {
      const float* ml = (const float*)(shm + L_RAW + 6144) + wc * 64 + 32 * (i >> 1) + 8 * q + 4 * (i & 1);
      const f32x4 t = *(const f32x4*)ml + *(const f32x4*)(ml + 1792);
      const unsigned u0 = pk(t[0], t[1]), u1 = pk(t[2], t[3]);
      const unsigned z0 = pk(bflo(u0) + 1.f, bfhi(u0) + 1.f), z1 = pk(bflo(u1) + 1.f, bfhi(u1) + 1.f);
      cav[i] = f32x4{bflo(z0), bfhi(z0), bflo(z1), bfhi(z1)};
    }
#pragma unroll
    for (int jh = 0; jh < 2; ++jh) {
      const int gq = (2 * wr + jh + dq + 1) & 3;
      const float sg = (gq >> 1) ? -1.f : 1.f;
#pragma unroll
      for (int ip = 0; ip < 2; ++ip) {
        const f32x4 c0 = cav[2 * ip] * sg, c1 = cav[2 * ip + 1] * sg;
#pragma unroll
        for (int jl = 0; jl < 2; ++jl) {
          const int j = 2 * jh + jl;
          const int u = j < 2 ? j : (j == 3 ? wc : 1 - wc);
          const f32x4 a0 = acc[2 * ip][j] * c0, a1 = acc[2 * ip + 1][j] * c1;
          v4u o; o.x = pk(a0[0], a0[1]); o.y = pk(a0[2], a0[3]); o.z = pk(a1[0], a1[1]); o.w = pk(a1[2], a1[3]);
          const int tt = 4 * p + 2 * (gq & 1) + u, kb = (gq >> 1) * 16 + 4 * dq + 2 * wc + ip;
          bst<SC1>(rs, l16, OFF_PQ + ((tt * 32 + kb) * 2 + kh) * 1024, o);
        }
      }
    }
    if (wr == 1 && q < 2) st1(rs, lane * 4, OFF_RS + (p * 2 + (dq >> 1)) * 1024 + ((2 * (dq & 1) + wc) * 2 + kh) * 128, asu((dq >> 1) ? -rsum[0] : rsum[0]));
  }
  STAMP_MID(11)
  lds_inc(cnt + 6, lane);
  WAIT_V0();
  if (lane == 0) flag_set(ws, rs, FLINE(LN_F1 + p) + (c * 4 + w) * 4, tag);
  STAMP_MID(2)
  SCHED();
  if (LATE_ST) {
    if (w < 2) {
      if (jb < 176) job_store();
      WAIT_V0();
      if (lds_inc(cnt + 14, lane) == 1u) { if (lane < 8) flag_set(ws, rs, FLINE(LN_G3 + lane * 8 + (b >> 5)) + (b & 31) * 4, tag); }
    }
    __builtin_amdgcn_s_sleep(LATE_SLEEP);
    wait_words<16>(rs, (lane >> 5) * FSTR + (lane & 31) * 4, FLINE(LN_G3 + xcd * 8 + w * 2), tag, true);
    lds_inc(cnt + 7, lane);
    STAMP_MID(15)
  }
  constexpr int WK = 11;
  v4u w1f[WK][3];
  f32x4 accU[3];
  unsigned a1[3];
#pragma unroll
  for (int i = 0; i < 3; ++i) a1[i] = OFF_W1 + (ft0 + (i < nft ? i : 0)) * NKB_X * 1024;
  auto ldw1 = [&](int k, v4u (&d)[3]) {
    const unsigned ko = ((k + 2 * c) & 15) * 1024;
#pragma unroll
    for (int i = 0; i < 3; ++i) d[i] = bld<SC1>(rs, l16, a1[i] + ko);
  };
  auto upd = [&](int k, v4u (&d)[3], bool first) {
    const int kb = (k + 2 * c) & 15;
    const int ad = m == 8 ? L_MB + (512 + kb * 32 + q * 8) * 2 : (m == (kb >> 1) ? L_MB + (1024 + kb * 32 + q * 8) * 2 : L_ZERO);
    const v4u cf = *(const v4u*)(shm + ad);
#pragma unroll
    for (int i = 0; i < 3; ++i) accU[i] = mfma(cf, d[i], first ? f32x4{0, 0, 0, 0} : accU[i]);
  };
  auto cfad = [&](int k) {
    const int kb = (k + 2 * c) & 15;
    return m == 8 ? L_MB + (512 + kb * 32 + q * 8) * 2 : (m == (kb >> 1) ? L_MB + (1024 + kb * 32 + q * 8) * 2 : L_ZERO);
  };
  wait_lds(cnt + 7, LATE_ST ? 8 : 4);
#pragma unroll
  for (int k = 0; k < W1_B1; ++k) ldw1(k, w1f[k]);
  SCHED();
#pragma unroll
  for (int k = 0; k < W1_B1; ++k) upd(k, w1f[k], k == 0);
  SCHED();
  STAMP_MID(10)
  wait_lds(cnt + 8, 4);
#pragma unroll
  for (int k = W1_B1; k < WK; ++k) ldw1(k, w1f[k]);
  SCHED();
  f32x4 acc[3][4];
  v4u hb[2][4], cf;
  auto rdh = [&](int k) {
#pragma unroll
    for (int j = 0; j < 4; ++j) hb[k & 1][j] = *(const v4u*)(shm + L_H2 + j * H2W + k * 1024 + l16);
  };
  BAR();
  SCHED();
  STAMP_MID(3)
  rdh(0);
#pragma unroll
  for (int k = 0; k < 16; ++k) {
    if (k == 3) { STAMP_MID(6) BAR(); STAMP_MID(7) }
    SCHED();
    rdh(k + 1);
    SCHED();
    if (k >= W1_B1) {
#pragma unroll
      for (int i = 0; i < 3; ++i) accU[i] = mfma(cf, w1f[k % WK][i], accU[i]);
      SCHED();
    }
    if (k + 1 >= W1_B1 && k + 1 < 16) { cf = *(const v4u*)(shm + cfad(k + 1)); SCHED(); }
#pragma unroll
    for (int j = 0; j < 4; ++j)
#pragma unroll
      for (int i = 0; i < 3; ++i) acc[i][j] = mfma(w1f[k % WK][i], hb[k & 1][j], k == 0 ? f32x4{0, 0, 0, 0} : acc[i][j]);
    if (k + WK < 16) { SCHED(); ldw1(k + WK, w1f[k]); }
    SCHED();
    if (k + WK == 15) { if (lane == 0) __hip_atomic_fetch_add((LAS unsigned*)cnt + 9, 1u, __ATOMIC_RELAXED, __HIP_MEMORY_SCOPE_WORKGROUP); }
  }
  v4u uf[3];
#pragma unroll
  for (int i = 0; i < 3; ++i) {
    const unsigned h0 = pk(accU[i][0], accU[i][1]), h1 = pk(accU[i][2], accU[i][3]);
    uf[i] = v4u{h0, h1, pk(accU[i][0] - bflo(h0), accU[i][1] - bfhi(h0)), pk(accU[i][2] - bflo(h1), accU[i][3] - bfhi(h1))};
  }
  float rsd[4];
#pragma unroll
  for (int j = 0; j < 4; ++j) rsd[j] = ((const float*)(shm + L_RSTD))[j * 16 + m];
#pragma unroll
  for (int i = 0; i < 3; ++i)
#pragma unroll
    for (int j = 0; j < 4; ++j) acc[i][j] = mfma(uf[i], hb[0][j], acc[i][j]);
  STAMP_MID(4)
  const unsigned vo = (q & 1) * (NKB_H * 1024) + ((q >> 1) * 16 + m) * 16;
#pragma unroll
  for (int i = 0; i < 3; ++i) {
    if (i < nft) {
      const int ft = ft0 + i;
#pragma unroll
      for (int j = 0; j < 4; j += 2) {
        const f32x4 a = acc[i][j] * rsd[j], bb = acc[i][j + 1] * rsd[j + 1];
        unsigned a0 = pk(fmaxf(a[0], 0.f), fmaxf(a[1], 0.f)), a1v = pk(fmaxf(a[2], 0.f), fmaxf(a[3], 0.f));
        unsigned b0 = pk(fmaxf(bb[0], 0.f), fmaxf(bb[1], 0.f)), b1 = pk(fmaxf(bb[2], 0.f), fmaxf(bb[3], 0.f));
        asm volatile("s_nop 1\n\tv_permlane16_swap_b32 %0, %2\n\tv_permlane16_swap_b32 %1, %3" : "+v"(a0), "+v"(a1v), "+v"(b0), "+v"(b1));
        bst<SC1>(rs, vo, OFF_H1 + ((4 * p + j) * NKB_H + (ft >> 1)) * 1024 + (ft & 1) * 512, v4u{a0, a1v, b0, b1});
      }
    }
  }
  STAMP_MID(12)
  lds_inc(cnt + 2, lane);
  WAIT_V0();
  if (lane == 0) flag_set(ws, rs, FLINE(LN_F3 + p * 4 + (c >> 1)) + ((c & 1) * 4 + w) * 4, tag);
  STAMP_MID(5)
  STAMP_END(w == 0 ? 1 : 5 + w, b, ws)
}

extern "C" void kernel_launch(void* const* d_in, const int* in_sizes, int n_in, void* d_out, int out_size, void* d_ws, size_t ws_size,
                              hipStream_t stream) {
  (void)hipFuncSetAttribute((const void*)fused_all, hipFuncAttributeMaxDynamicSharedMemorySize, K_LDS);
  fused_all<<<256, 512, K_LDS, stream>>>((const char*)d_in[0], (const char*)d_in[1], (const char*)d_in[2], (const char*)d_in[3], (const char*)d_in[4],
                                         (const char*)d_in[6], (const char*)d_in[7], (const char*)d_in[8], (const char*)d_in[9], (char*)d_ws, (char*)d_out);
}
```

```cpp
#include <hip/hip_runtime.h>
#include <hip/hip_bf16.h>
#include <stdint.h>

typedef unsigned v4u __attribute__((ext_vector_type(4)));
typedef unsigned v2u __attribute__((ext_vector_type(2)));
typedef float f32x4 __attribute__((ext_vector_type(4)));
typedef float f32x2 __attribute__((ext_vector_type(2)));
typedef short bf16x8 __attribute__((ext_vector_type(8)));
typedef __bf16 bf2 __attribute__((ext_vector_type(2)));
#define GAS __attribute__((address_space(1)))
#define LAS __attribute__((address_space(3)))
#define WAIT_V(n) asm volatile("s_waitcnt vmcnt(%0)" ::"n"(n) : "memory")
#define CFENCE() asm volatile("" ::: "memory")
#define WAIT_L0() do { CFENCE(); __builtin_amdgcn_s_waitcnt(0xC07F); } while (0)
#define WAIT_V0() do { CFENCE(); __builtin_amdgcn_s_waitcnt(0x0F70); CFENCE(); } while (0)
#define SCHED() __builtin_amdgcn_sched_barrier(0)
#define BAR() do { CFENCE(); __builtin_amdgcn_s_barrier(); CFENCE(); } while (0)
#define RSRC(p, n) __builtin_amdgcn_make_buffer_rsrc((void*)(p), 0, (n), 0x00020000)
constexpr int SC1 = 16, NTP = 2;
#define ABL 0

#define STAMP_BEGIN()
#define STAMP_MID(i) {}
#define STAMP_END(k, blk, ws) {}

constexpr int D = 512, F = 1376;
constexpr int NKB_X = 16, NKB_H = 43;
constexpr unsigned OFF_EPOCH = 0;
#define PRIV 0
#define FSTR 256
constexpr unsigned OFF_FL   = 24u << 20;
constexpr unsigned LN_G1 = 0;
constexpr unsigned LN_G2 = 64;
constexpr unsigned LN_F1 = 128;
constexpr unsigned LN_F3 = 160;
constexpr unsigned LN_F2 = 288;
constexpr unsigned LN_G3 = 416;
constexpr unsigned LN_P1 = 480;
constexpr unsigned LN_P2 = LN_P1 + 256;
constexpr unsigned LN_P3 = LN_P2 + 256;
constexpr unsigned LN_END = LN_P3 + 256;
#define FLINE(l) (OFF_FL + (l) * FSTR)
constexpr unsigned OFF_MOD  = 49152;
constexpr unsigned OFF_RS   = 131072;
constexpr unsigned OFF_ST   = 196608;
constexpr unsigned OFF_XNT  = 524288;
constexpr unsigned OFF_WD   = OFF_XNT + (2u << 20);
constexpr unsigned OFF_W1   = OFF_WD + (1u << 20);
constexpr unsigned OFF_W2   = OFF_W1 + 86 * 16 * 1024;
constexpr unsigned OFF_PQ   = OFF_W2 + 32 * 43 * 1024;
constexpr unsigned OFF_X1   = OFF_PQ + (8u << 20);
constexpr unsigned OFF_H1   = OFF_X1 + (2u << 20);
constexpr unsigned WS_BYTES = FLINE(LN_END);

template <int I> struct IC { static constexpr int v = I; };
template <int B, int E, class Fn> __device__ __forceinline__ void static_for(Fn&& f) { if constexpr (B < E) { f(IC<B>{}); static_for<B + 1, E>(f); } }
__device__ __forceinline__ float asf(unsigned u) { return __builtin_bit_cast(float, u); }
__device__ __forceinline__ unsigned asu(float f) { return __builtin_bit_cast(unsigned, f); }
__device__ __forceinline__ float bflo(unsigned u) { return asf(u << 16); }
__device__ __forceinline__ float bfhi(unsigned u) { return asf(u & 0xffff0000u); }
__device__ __forceinline__ unsigned pk(float lo, float hi) {
  f32x2 v = {lo, hi};
  return __builtin_bit_cast(unsigned, __builtin_convertvector(v, bf2));
}
__device__ __forceinline__ float rbf(float x) { return bflo(pk(x, x)); }
__device__ __forceinline__ f32x4 mfma(v4u a, v4u b, f32x4 c) {
  return __builtin_amdgcn_mfma_f32_16x16x32_bf16(__builtin_bit_cast(bf16x8, a), __builtin_bit_cast(bf16x8, b), c, 0, 0, 0);
}
template <int CTRL> __device__ __forceinline__ float dpp_add(float x) {
  int y = __builtin_amdgcn_update_dpp(0, __builtin_bit_cast(int, x), CTRL, 0xf, 0xf, false);
  return x + __builtin_bit_cast(float, y);
}
__device__ __forceinline__ float wave_sum(float x) {
  x = dpp_add<0x111>(x); x = dpp_add<0x112>(x); x = dpp_add<0x114>(x); x = dpp_add<0x118>(x);
  x = dpp_add<0x142>(x); x = dpp_add<0x143>(x);
  return __builtin_bit_cast(float, __builtin_amdgcn_readlane(__builtin_bit_cast(int, x), 63));
}
__device__ __forceinline__ float quad16_sum(float x) {
  float y = x;
  asm volatile("s_nop 1\n\tv_permlane16_swap_b32 %0, %1" : "+v"(x), "+v"(y));
  x += y;
  y = x;
  asm volatile("s_nop 1\n\tv_permlane32_swap_b32 %0, %1" : "+v"(x), "+v"(y));
  return x + y;
}
__device__ __forceinline__ int fresh_lane() { int l; asm volatile("v_mbcnt_lo_u32_b32 %0, -1, 0\n\tv_mbcnt_hi_u32_b32 %0, -1, %0" : "=v"(l)); return l; }
__device__ __forceinline__ v4u ldnt(const void* p) { return __builtin_nontemporal_load((const v4u*)p); }
template <int AUX> __device__ __forceinline__ v4u bld(__amdgpu_buffer_rsrc_t rs, unsigned voff, unsigned soff) { return __builtin_amdgcn_raw_buffer_load_b128(rs, voff, soff, AUX); }
template <int AUX> __device__ __forceinline__ void bst(__amdgpu_buffer_rsrc_t rs, unsigned voff, unsigned soff, v4u v) { __builtin_amdgcn_raw_buffer_store_b128(v, rs, voff + soff, 0, AUX); }
__device__ __forceinline__ void st1(__amdgpu_buffer_rsrc_t rs, unsigned voff, unsigned soff, unsigned v) { __builtin_amdgcn_raw_buffer_store_b32(v, rs, voff, soff, SC1); }
#define FLAG_MODE 1
__device__ __forceinline__ void flag_set(char* ws, __amdgpu_buffer_rsrc_t rs, unsigned off, unsigned v) {
  asm volatile("global_atomic_swap %0, %1, %2 sc1" :: "v"(off), "v"(v), "s"(ws) : "memory");
}
__device__ __forceinline__ unsigned ld1(__amdgpu_buffer_rsrc_t rs, unsigned voff, unsigned soff) { return __builtin_amdgcn_raw_buffer_load_b32(rs, voff, soff, SC1); }
template <int AUX> __device__ __forceinline__ void glds16(const char* g, LAS char* l) {
  __builtin_amdgcn_global_load_lds((const GAS unsigned*)g, (LAS unsigned*)l, 16, 0, AUX);
}

#define AUX_OUT 0
#define JOB_LATE 1
#define W_PRE 4
#define WD_C 12
#define W1_B1 12
#define W1_TRIG 13
#define KW 0
#define AUXW0 0
#define AUXW1 0
#define NREP 8
#define G1_SLEEP 2
#define W_EARLY 0
#define W_HOLD 0
#define JOBPERM 1
#define LATE_ST 1
#define LATE_SLEEP 64
#define AUXJ 0
#define XNT ""
#define PQ_WIN 12
#define H_WIN 8
#define W1_LEAD 0
#define P4MIX 0
#define GET3 58
#define GET5 40
#define PRIO 0
#define XS1 0
#define XS2 0
#define XS3 0
#define TOUCH 0
#define F2_PRE 10
#define F3_PRE 14
#define LATE_TRIG 40
#define G1_PRE 0
#define F3_AD 1
#define F1_PRE 0
#define AUX_X 16
#define WD_A 0
#define WD_B 12
#define W1_NB1 8
#define W1_AH 2
#define POLL_SLEEP 2
constexpr int NS = 4, STAGE = 32768;
constexpr int L_TOP = NS * STAGE;
constexpr int L_RAW = L_TOP;
constexpr int L_MB = L_RAW + 14336;
constexpr int L_SL = L_MB + 3072;
constexpr int L_ZERO = L_TOP + 24576;
constexpr int L_CNT = L_ZERO + 64;
constexpr int L_RSTD = L_CNT + 64;
constexpr int K_LDS = L_RSTD + 256;
constexpr int L_LN = 3 * STAGE;
constexpr int L_H2 = 0, H2W = 17 * 1024;
constexpr int L_W1X = 4 * H2W;
constexpr unsigned SPIN_CAP = 1u << 16;

template <int SLP = POLL_SLEEP>
__device__ __forceinline__ void wait_words(__amdgpu_buffer_rsrc_t rs, unsigned voff, unsigned soff, unsigned tag, bool sel) {
#pragma unroll 1
  for (unsigned spins = 0; spins < SPIN_CAP; ++spins) {
    CFENCE();
    const unsigned v = ld1(rs, voff, soff);
    if (__builtin_amdgcn_ballot_w64(sel && v != tag) == 0ull) break;
    __builtin_amdgcn_s_sleep(SLP);
  }
  __builtin_amdgcn_fence(__ATOMIC_ACQUIRE, "wavefront");
  CFENCE();
  SCHED();
}
__device__ __forceinline__ void wait_lds(volatile LAS unsigned* p, unsigned n) {
#pragma unroll 1
  for (unsigned spins = 0; spins < SPIN_CAP * 64; ++spins) {
    if (__builtin_amdgcn_readfirstlane(*p) >= n) break;
    __builtin_amdgcn_s_sleep(1);
  }
  CFENCE();
  SCHED();
}
__device__ __forceinline__ void wait_lds2(volatile LAS unsigned* p, unsigned n) {
  const unsigned a = (unsigned)(uintptr_t)p;
#pragma unroll 1
  for (unsigned spins = 0; spins < SPIN_CAP * 64; ++spins) {
    unsigned v;
    asm volatile("ds_read_b32 %0, %1\n\ts_waitcnt lgkmcnt(0)" : "=v"(v) : "v"(a) : "memory");
    if (__builtin_amdgcn_readfirstlane(v) >= n) break;
    __builtin_amdgcn_s_sleep(1);
  }
  SCHED();
}
__device__ __forceinline__ void wait_lds0(volatile LAS unsigned* p, unsigned n) {
#pragma unroll 1
  for (unsigned spins = 0; spins < SPIN_CAP * 64; ++spins) { if (__builtin_amdgcn_readfirstlane(*p) >= n) break; }
  CFENCE();
  SCHED();
}
__device__ __forceinline__ void lds_add(int idx, int) {
  unsigned long long ex;
  asm volatile("s_mov_b64 %0, exec\n\ts_mov_b64 exec, 1\n\tds_add_u32 %1, %2\n\ts_mov_b64 exec, %0" : "=&s"(ex) : "v"(L_CNT + idx * 4), "v"(1u) : "memory");
}
__device__ __forceinline__ unsigned lds_inc(volatile LAS unsigned* p, int lane) {
  unsigned old = 0;
  if (lane == 0) old = __hip_atomic_fetch_add((LAS unsigned*)p, 1u, __ATOMIC_RELAXED, __HIP_MEMORY_SCOPE_WORKGROUP);
  return __builtin_amdgcn_readfirstlane(old);
}

__global__ __launch_bounds__(512, 2) void fused_all(const char* __restrict__ x, const char* __restrict__ wsr, const char* __restrict__ wsi,
                                                    const char* __restrict__ wdr, const char* __restrict__ wdi,
                                                    const char* __restrict__ w1, const char* __restrict__ w2, const char* __restrict__ cvec,
                                                    const char* __restrict__ wmod, char* __restrict__ ws, char* __restrict__ out) {
  extern __shared__ __attribute__((aligned(16))) char shm[];
  STAMP_BEGIN()
  const int b = blockIdx.x, xcd = b & 7, idx = b >> 3, pl = idx >> 3, p = xcd * 4 + pl, c = idx & 7, dq = c & 3, kh = c >> 2;
  const int w8 = __builtin_amdgcn_readfirstlane(threadIdx.x >> 6), w = w8 & 3;
  const __amdgpu_buffer_rsrc_t rs = RSRC(ws, WS_BYTES);
  const float* sll = (const float*)(shm + L_SL);
  volatile LAS unsigned* cnt = (volatile LAS unsigned*)((LAS char*)shm + L_CNT);

  v4u xq; unsigned tq;
  asm volatile("global_load_dwordx4 %0, %2, %3" XNT "\n\tglobal_load_dword %1, %4, %5 sc1"
               : "=&v"(xq), "=&v"(tq) : "v"(b * 8192 + threadIdx.x * 16), "s"(x), "v"(OFF_EPOCH), "s"(ws) : "memory");
  SCHED();
  v4u R[8], cvv;
  const int jb = c * 32 + pl * 8 + xcd;
  const int jm = ((c * 2 + (pl >> 1)) * 8 + xcd) * 2 + (pl & 1);
  const bool is1 = w == 0;
  const int jk0 = (is1 ? (jb & 7) : (jb >> 3)) * 64, jn0 = (is1 ? (jb >> 3) : (jb & 7)) * 64;
  {
    const int lane = threadIdx.x & 63, m = lane & 15, q = lane >> 4, kg = lane >> 3, ng = lane & 7;
    const char* base; unsigned off, ld;
    if (w8 >= 4 || (w >= 2 ? jm >= 192 : jb >= 240)) { base = cvec; ld = 0; off = 0; }
    else if (w >= 2) { base = wmod; ld = 6144; off = ((jm / 96) * 256 + (w & 1) * 128 + (lane >> 2) * 8) * 6144 + (jm % 96) * 64 + (lane & 3) * 16; }
    else if (jb < 176) {
      base = is1 ? w1 : w2; ld = is1 ? F * 2 : D * 2;
      const bool ok = (jk0 + kg * 8 < (is1 ? D : F)) && (jn0 + ng * 8 < (is1 ? F : D));
      off = ok ? (jk0 + kg * 8) * ld + (jn0 + ng * 8) * 2 : 0;
    } else {
      const int j3 = w * 64 + jb - 176, et = j3 >> 2, kb0 = (j3 & 3) * 8;
      base = kb0 < 16 ? wdr : wdi; ld = 64;
      off = (32 * (et >> 1) + 8 * (m >> 2) + 4 * (et & 1) + (m & 3)) * 1024 + (kb0 & 15) * 64 + q * 16;
    }
    ld = __builtin_amdgcn_readfirstlane(ld);
    const __amdgpu_buffer_rsrc_t rj = RSRC(base, 0x7fffffff);
#pragma unroll
    for (int k = 0; k < 8; ++k) R[k] = bld<AUXJ>(rj, off, k * ld);
    cvv = bld<0>(RSRC(cvec, 1024), ((w & 1) * 128 + (lane >> 2) * 8) * 2, (jm / 96 & 1) * 512);
    SCHED();
  }
  if (w8 < 4 || W_EARLY == 0) {
    WAIT_V(9);
  } else {
    const int lane = threadIdx.x & 63;
    if (W_EARLY > 0) {
      const int gq = (w + dq + 1) & 3;
      const char* src = (gq < 2 ? wsr : wsi) + (size_t)(p * 64 + (gq & 1) * 32 + (lane >> 3)) * 4096 + kh * 2048;
      const int sl = lane & 7, h = lane >> 4;
#pragma unroll
      for (int st = 0; st < W_EARLY; ++st)
#pragma unroll
        for (int i = 0; i < 4; ++i)
          glds16<AUXW0>(src + ((sl ^ (4 * (i & 1) + h)) * 16) + i * 8 * 4096 + st * 128, (LAS char*)shm + st * STAGE + w * 4096 + 16384 + i * 1024);
    }
    SCHED();
    WAIT_V(9 + 4 * W_EARLY);
  }
  asm volatile("" : "+v"(xq), "+v"(tq) :: "memory");
  unsigned tag;
  {
    const int tid = threadIdx.x, lane = tid & 63;
    float v[8];
#pragma unroll
    for (int u = 0; u < 4; ++u) { v[2 * u] = bflo(xq[u]); v[2 * u + 1] = bfhi(xq[u]); }
    const float mean = wave_sum(((v[0] + v[1]) + (v[2] + v[3])) + ((v[4] + v[5]) + (v[6] + v[7]))) * (1.f / D);
    float ss = 0.f;
#pragma unroll
    for (int j = 0; j < 8; ++j) { const float t = v[j] - mean; ss += t * t; }
    const float var = wave_sum(ss) * (1.f / D);
    const float mb = rbf(mean);
    const float rsd = 1.f / rbf(__builtin_sqrtf(rbf(rbf(var) + 1e-5f)));
    v4u o;
#pragma unroll
    for (int u = 0; u < 4; ++u) {
      const unsigned d2 = pk(v[2 * u] - mb, v[2 * u + 1] - mb);
      o[u] = pk(bflo(d2) * rsd, bfhi(d2) * rsd);
    }
    *(v4u*)(shm + L_LN + tid * 16) = o;
    if (tid < 32) ((unsigned*)(shm + L_ZERO))[tid] = 0u;
    WAIT_L0();
    BAR();
    const int d = (tid & ~31) + 8 * ((tid >> 2) & 3) + 4 * ((tid >> 4) & 1) + (tid & 3);
    const unsigned short* sp = (const unsigned short*)(shm + L_LN) + d;
    v4u t;
#pragma unroll
    for (int u = 0; u < 4; ++u) t[u] = (unsigned)sp[(2 * u) * 512] | ((unsigned)sp[(2 * u + 1) * 512] << 16);
    tag = __builtin_amdgcn_readfirstlane(tq) + 1u;
    bst<SC1>(rs, tid * 16, OFF_XNT + b * 8192, t);
    WAIT_V0();
    BAR();
    if (w8 == 0) { if (lane < NREP) flag_set(ws, rs, FLINE(LN_G1 + lane * 8 + (b >> 5)) + (b & 31) * 4, tag); }
    else __builtin_amdgcn_s_sleep(1);
  }
  if (w8 >= 4) {
    const int lane = fresh_lane(), m = lane & 15, q = lane >> 4;
    const unsigned l16 = lane * 16;
    v4u wd[8][4], res[2];
    {
      const int gq = (w + dq + 1) & 3;
      const char* wbase = (gq < 2 ? wsr : wsi) + (size_t)(p * 64 + (gq & 1) * 32 + (lane >> 3)) * 4096 + kh * 2048;
      const int sl = lane & 7, h = lane >> 4;
      const char* wsrc0 = wbase + ((sl ^ h) * 16);
      const char* wsrc1 = wbase + ((sl ^ (4 + h)) * 16) + 8 * 4096;
      const char* xsrc = ws + OFF_XNT + ((size_t)(kh * 128 + 2 * w) * 512 + dq * 128 + lane) * 16;
      auto issW0 = [&](int st) {
        LAS char* dst = (LAS char*)shm + (st & 3) * STAGE + w * 4096 + 16384;
        const int kk = (st + KW * dq) & 15;
#pragma unroll
        for (int i = 0; i < 4; ++i) glds16<AUXW0>(((i & 1) ? wsrc1 : wsrc0) + (size_t)(i >> 1) * 16 * 4096 + kk * 128, dst + i * 1024);
      };
      auto issW = [&](int st) {
        LAS char* dst = (LAS char*)shm + (st & 3) * STAGE + w * 4096 + 16384;
        const int kk = (st + KW * dq) & 15;
#pragma unroll
        for (int i = 0; i < 4; ++i) glds16<AUXW1>(((i & 1) ? wsrc1 : wsrc0) + (size_t)(i >> 1) * 16 * 4096 + kk * 128, dst + i * 1024);
      };
      auto issX = [&](int st) {
        LAS char* dst = (LAS char*)shm + (st & 3) * STAGE + w * 4096;
#pragma unroll
        for (int i = 0; i < 4; ++i) glds16<AUX_X>(xsrc + (size_t)((st + KW * dq) & 15) * 65536 + (i >> 1) * 8192 + (i & 1) * 1024, dst + i * 1024);
      };
      STAMP_MID(13)
      if (W_HOLD) wait_lds2(cnt + 12, 4);
#pragma unroll
      for (int i = W_EARLY; i < W_PRE; ++i) issW0(i);
#pragma unroll
      for (int ip = 0; ip < 2; ++ip) res[ip] = bld<AUXJ>(RSRC(x, 2048 * 1024), m * 1024 + q * 16 + ip * 64, (p * 64 + w * 16) * 1024 + c * 128);
      SCHED();
      if (w < 2) { if (G1_PRE) { WAIT_V(0); __builtin_amdgcn_s_sleep(G1_PRE); } wait_words<G1_SLEEP>(rs, (lane >> 5) * FSTR + (lane & 31) * 4, FLINE(LN_G1 + (xcd % NREP) * 8 + kh * 4 + w * 2), tag, true); lds_inc(cnt + 1, lane); }
      wait_lds(cnt + 1, 2);
      STAMP_MID(14)
      issX(0); issX(1);
#pragma unroll
      for (int i = W_PRE; i < 4; ++i) issW0(i);
      issX(2); issX(3);
      WAIT_V(12 + 4 * (4 - W_PRE)); BAR();
      WAIT_V(8 + 4 * (4 - W_PRE)); BAR();
      issX(4);
      WAIT_V(8); BAR();
      issW(4); issW(5);
      WAIT_V(12); BAR();
      issX(5); issX(6);
#pragma unroll 1
      for (int t = 3; t < 11; t += 2) {
        WAIT_V(12); BAR();
        issW(t + 3); issW(t + 4);
        WAIT_V(12); BAR();
        issX(t + 4); issX(t + 5);
      }
      WAIT_V(12); BAR();
      issW(14); issW(15);
      STAMP_MID(0)
      const unsigned a0 = OFF_WD + ((4 * c) * 32 + 8 * w) * 1024;
      auto ldwd = [&](int f0, int f1) {
        SCHED();
#pragma unroll
        for (int f = f0; f < f1; ++f) wd[f >> 2][f & 3] = bld<SC1>(rs, l16, a0 + ((f & 3) * 32 + (f >> 2)) * 1024);
        SCHED();
      };
      WAIT_V(12); BAR();
      issX(15);
      {
        const char* mp = ws + OFF_MOD + (w & 1) * 12288;
        LAS char* dst = (LAS char*)shm + L_RAW + (w & 1) * 7168;
        const unsigned sl_off = lane < 32 ? 2048 + dq * 512 + l16 : (lane < 48 ? 4096 + c * 256 + l16 - 512 : 10240 + c * 256 + l16 - 768);
#pragma unroll
        for (int i = 0; i < 4; ++i) {
          const int pc = (w >> 1) * 4 + i;
          const unsigned so = pc < 2 ? pc * 1024 + l16 : (pc < 6 ? 4096 + pc * 1024 + l16 : sl_off);
          glds16<SC1>(mp + so, dst + (pc < 6 ? pc : 6) * 1024);
        }
      }
      ldwd(0, WD_A);
      WAIT_V(8 + 4 + WD_A); BAR();
      ldwd(WD_A, WD_A + WD_B);
      WAIT_V(WD_A + WD_B); BAR();
      ldwd(WD_A + WD_B, WD_A + WD_B + WD_C);
      {
        const int tid = w * 64 + lane;
        f32x2 pa[3], pb[3];
        float sa1, sb1;
        asm volatile("ds_read_b64 %0, %8\n\tds_read_b64 %1, %8 offset:2048\n\tds_read_b64 %2, %8 offset:4096\n\t"
                     "ds_read_b64 %3, %8 offset:7168\n\tds_read_b64 %4, %8 offset:9216\n\tds_read_b64 %5, %8 offset:11264\n\t"
                     "ds_read_b32 %6, %9\n\tds_read_b32 %7, %9 offset:7168\n\ts_waitcnt lgkmcnt(0)"
                     : "=&v"(pa[0]), "=&v"(pa[1]), "=&v"(pa[2]), "=&v"(pb[0]), "=&v"(pb[1]), "=&v"(pb[2]), "=&v"(sa1), "=&v"(sb1)
                     : "v"(L_RAW + tid * 8), "v"(tid < 128 ? L_RAW + 6656 + tid * 4 : L_RAW + 4096 + c * 256 + (tid & 63) * 4) : "memory");
        unsigned z[3];
#pragma unroll
        for (int i = 0; i < 3; ++i) {
          z[i] = pk(pa[i][0] + pb[i][0], pa[i][1] + pb[i][1]);
          if (i == 2) z[i] = pk(bflo(z[i]) + 1.f, bfhi(z[i]) + 1.f);
        }
        float sv = rbf(sa1 + sb1);
        if (tid >= 128) sv = rbf(sv + 1.f);
        asm volatile("ds_write_b32 %0, %1\n\tds_write_b32 %0, %2 offset:1024\n\tds_write_b32 %0, %3 offset:2048\n\tds_write_b32 %0, %4 offset:3072\n\ts_waitcnt lgkmcnt(0)"
                     :: "v"(L_MB + tid * 4), "v"(z[0]), "v"(z[1]), "v"(z[2]), "v"(sv) : "memory");
        lds_add(13, lane);
      }
      if (WD_A + WD_B + WD_C < 32) { wait_lds2(cnt + 6, 4); ldwd(WD_A + WD_B + WD_C, 32); }
      wait_lds2(cnt + 13, 4);
    }
    SCHED();
    f32x4 acc[4], oth[4];
    f32x4* red = (f32x4*)(shm + L_H2);
    auto put = [&](int j, int ln) {
#pragma unroll
      for (int i = 0; i < 4; ++i) red[((j + w) & 3) * (H2W / 16) + ((j - 1) * 4 + i) * 64 + ln] = acc[i];
    };
    auto get = [&](int ln) {
#pragma unroll
      for (int i = 0; i < 4; ++i)
        oth[i] = (red[w * (H2W / 16) + i * 64 + ln] + red[w * (H2W / 16) + (4 + i) * 64 + ln]) + red[w * (H2W / 16) + (8 + i) * 64 + ln];
    };
    {
      f32x4 accS[4];
      v4u sf[8];
#pragma unroll
      for (int k = 0; k < 8; ++k) sf[k] = *(const v4u*)(shm + L_MB + ((8 * (w & 1) + k) * 32 + q * 8) * 2);
      SCHED();
#pragma unroll
      for (int k = 0; k < 8; ++k)
#pragma unroll
        for (int i = 0; i < 4; ++i) accS[i] = mfma(wd[k][i], sf[k], k == 0 ? f32x4{0, 0, 0, 0} : accS[i]);
      SCHED();
      if (PRIO) __builtin_amdgcn_s_setprio(1);
      STAMP_MID(1)
      if (w == 0) { if (F1_PRE) __builtin_amdgcn_s_sleep(F1_PRE); wait_words(rs, (lane & 31) * 4, (PRIV & 1) ? FLINE(LN_P1 + p * 8 + c) : FLINE(LN_F1 + p), tag, true); lds_inc(cnt + 4, lane); }
      else wait_lds(cnt + 4, 1);
      STAMP_MID(2)
      f32x2 rr[4];
#pragma unroll
      for (int j = 0; j < 4; ++j)
#pragma unroll
        for (int h = 0; h < 2; ++h) rr[j][h] = asf(ld1(rs, m * 4 + h * 128, OFF_RS + (p * 2 + (w >> 1)) * 1024 + ((j + w) & 3) * 256));
      constexpr int WIN = PQ_WIN;
      v4u pq[WIN];
      unsigned b0[4];
#pragma unroll
      for (int j = 0; j < 4; ++j) b0[j] = OFF_PQ + ((4 * p + ((j + w) & 3)) * 64 + 16 * w) * 1024;
      auto ldp = [&](int n) { pq[n % WIN] = bld<SC1>(rs, l16, b0[3 - (n >> 4)] + (n & 15) * 1024); };
#pragma unroll
      for (int n = 0; n < WIN; ++n) ldp(n);
      SCHED();
      static_for<0, 64>([&](auto ic) __attribute__((always_inline)) {
        constexpr int n = decltype(ic)::v, j = 3 - (n >> 4), g = n & 15;
        const v4u cur = pq[n % WIN];
        if (g == 0 && j < 3) {
          f32x4 nx[4];
#pragma unroll
          for (int i = 0; i < 4; ++i) nx[i] = mfma(wd[0][i], cur, accS[i] * (rr[j][0] + rr[j][1]));
          put(j + 1, lane);
          if (j == 0) lds_add(10, lane);
#pragma unroll
          for (int i = 0; i < 4; ++i) acc[i] = nx[i];
        } else {
#pragma unroll
          for (int i = 0; i < 4; ++i) acc[i] = mfma(wd[g >> 1][i], cur, n == 0 ? accS[i] * (rr[j][0] + rr[j][1]) : acc[i]);
        }
        SCHED();
        if (n + WIN < 64) ldp(n + WIN);
        if (n + WIN == 63 - W1_LEAD) lds_add(7, lane);
        if (LATE_TRIG && n == LATE_TRIG) lds_add(0, lane);
        if (n == GET3) { asm volatile("" : "+v"(acc[0]), "+v"(acc[1]), "+v"(acc[2]), "+v"(acc[3])); wait_lds0(cnt + 10, 4); get(lane); }
        SCHED();
      });
    }
    STAMP_MID(3)
    auto gate = [&](const f32x4 (&gv)[4]) {
#pragma unroll
      for (int i = 0; i < 4; ++i) {
        const f32x4 t = (acc[i] + oth[i]) * gv[i];
        const unsigned r0 = res[i >> 1][2 * (i & 1)], r1 = res[i >> 1][2 * (i & 1) + 1];
        res[i >> 1][2 * (i & 1)] = pk(bflo(r0) + t[0], bfhi(r0) + t[1]);
        res[i >> 1][2 * (i & 1) + 1] = pk(bflo(r1) + t[2], bfhi(r1) + t[3]);
      }
    };
    const unsigned xb = OFF_X1 + (4 * p + w) * NKB_X * 1024, sb0 = OFF_ST + (p * 4 + w) * 1024;
    {
      f32x4 cv[4], gv[4];
#pragma unroll
      for (int i = 0; i < 4; ++i) { gv[i] = *(const f32x4*)(sll + 32 * (i >> 1) + 8 * q + 4 * (i & 1)); cv[i] = *(const f32x4*)(sll + 128 + 32 * (i >> 1) + 8 * q + 4 * (i & 1)); }
      gate(gv);
      STAMP_MID(10)
      float v[16];
#pragma unroll
      for (int u = 0; u < 8; ++u) { v[2 * u] = bflo(res[u >> 2][u & 3]); v[2 * u + 1] = bfhi(res[u >> 2][u & 3]); }
      float s = 0.f;
#pragma unroll
      for (int u = 0; u < 16; u += 4) s += (v[u] + v[u + 1]) + (v[u + 2] + v[u + 3]);
      const float mc = quad16_sum(s) * (1.f / 64);
#pragma unroll
      for (int u = 0; u < 16; ++u) v[u] -= mc;
#pragma unroll
      for (int ip = 0; ip < 2; ++ip) {
        v4u o;
#pragma unroll
        for (int u = 0; u < 4; ++u) o[u] = pk(v[8 * ip + 2 * u] * cv[2 * ip + (u >> 1)][(2 * u) & 3], v[8 * ip + 2 * u + 1] * cv[2 * ip + (u >> 1)][(2 * u + 1) & 3]);
        bst<SC1>(rs, l16, xb + (2 * c + ip) * 1024, o);
        *(v4u*)(shm + L_H2 + w * H2W + ip * 1024 + l16) = o;
      }
      float ss = 0.f, s2 = 0.f;
#pragma unroll
      for (int u = 0; u < 16; u += 2) { ss += v[u] * v[u]; s2 += v[u + 1] * v[u + 1]; }
      ss = quad16_sum(ss + s2);
      if (q == 0) __builtin_amdgcn_raw_buffer_store_b64(v2u{asu(mc), asu(ss)}, rs, m * 8, sb0 + c * 128, SC1);
      STAMP_MID(11)
      WAIT_V0();
      if (PRIV & 2) { if (lane < 8) flag_set(ws, rs, FLINE(LN_P2 + p * 8 + lane) + (w * 8 + c) * 4, tag); }
      else if (lane == 0) flag_set(ws, rs, FLINE(LN_F2 + p * 4 + w) + c * 4, tag);
      lds_inc(cnt + 8, lane);
    }
    asm volatile("" : "+v"(res[0]), "+v"(res[1]));
    SCHED();
    STAMP_MID(4)
    if (F2_PRE) __builtin_amdgcn_s_sleep(F2_PRE);
    wait_words(rs, (lane & 7) * 4 + (XS2 ? ((lane >> 3) & 3) * 4 * FSTR : 0), (PRIV & 2) ? FLINE(LN_P2 + p * 8 + c) + w * 32 : FLINE(LN_F2 + (XS2 ? xcd * 4 : p) * 4 + w), tag, true);
    STAMP_MID(5)
    {
      v4u X[14];
      v2u st[8];
#pragma unroll
      for (int k = 0; k < 14; ++k) X[k] = bld<SC1>(rs, l16, xb + ((2 * c + 2 + k) & 15) * 1024);
#pragma unroll
      for (int i = 0; i < 8; ++i) st[i] = __builtin_amdgcn_raw_buffer_load_b64(rs, m * 8 + i * 128, sb0, SC1);
      SCHED();
#pragma unroll
      for (int k = 0; k < 14; ++k) {
        *(v4u*)(shm + L_H2 + w * H2W + (2 + k) * 1024 + l16) = X[k];
        if (k == 1) { WAIT_L0(); BAR(); SCHED(); }
      }
      float mu[8], mean = 0.f, m2 = 0.f;
#pragma unroll
      for (int i = 0; i < 8; ++i) { mu[i] = asf(st[i].x); mean += mu[i]; m2 += asf(st[i].y); }
      mean *= 0.125f;
      float dv = 0.f;
#pragma unroll
      for (int i = 0; i < 8; ++i) { const float d0 = mu[i] - mean; dv += d0 * d0; }
      m2 += 64.f * dv;
      const float den = rbf(__builtin_sqrtf(rbf(rbf(m2 * (1.f / D)) + 1e-5f)));
      const float mb = rbf(mean);
      unsigned d0 = q == 0 ? pk(mu[0] - mb, mu[1] - mb) : pk(mu[4] - mb, mu[5] - mb);
      unsigned d1 = q == 0 ? pk(mu[2] - mb, mu[3] - mb) : pk(mu[6] - mb, mu[7] - mb);
      if (q >= 2) { d0 = q == 2 ? pk(den, 0.f) : 0u; d1 = 0u; }
      *(v4u*)(shm + L_H2 + w * H2W + 16 * 1024 + l16) = v4u{d0, d1, d0, d1};
      if (q == 0) ((float*)(shm + L_RSTD))[w * 16 + m] = 1.f / den;
      WAIT_L0(); BAR(); SCHED();
    }
    STAMP_MID(6)
    {
      constexpr int WIN = H_WIN;
      v4u w2f[11][4];
      const unsigned a0 = OFF_W2 + ((4 * c) * NKB_H + 11 * w) * 1024;
      const unsigned last = w == 3 ? 9 * 1024 : 10 * 1024;
#pragma unroll
      for (int k = 0; k < 11; ++k)
#pragma unroll
        for (int i = 0; i < 4; ++i) w2f[k][i] = bld<SC1>(rs, l16, a0 + i * NKB_H * 1024 + (k < 10 ? k * 1024 : last));
      SCHED();
      if (w == 3) {
#pragma unroll
        for (int i = 0; i < 4; ++i) w2f[10][i] = v4u{0, 0, 0, 0};
      }
      wait_lds(cnt + 2, 4);
      const int ln = fresh_lane();
      if (F3_PRE) __builtin_amdgcn_s_sleep(F3_PRE);
      wait_words(rs, (ln & 7) * 4 + (XS3 ? ((ln >> 3) & 3) * 4 * FSTR : 0), (PRIV & 4) ? FLINE(LN_P3 + p * 8 + c) + w * 32 : FLINE(LN_F3 + (XS3 ? xcd * 4 : p) * 4 + w), tag, true);
      STAMP_MID(7)
      v4u hf[WIN];
      unsigned b0[4];
#pragma unroll
      for (int j = 0; j < 4; ++j) b0[j] = OFF_H1 + ((4 * p + ((j + w) & 3)) * NKB_H + 11 * w) * 1024;
      auto ldh = [&](int n) { hf[n % WIN] = bld<SC1>(rs, ln * 16, b0[3 - n / 11] + (n % 11 < 10 ? (n % 11) * 1024 : last)); };
#pragma unroll
      for (int n = 0; n < WIN; ++n) ldh(n);
      SCHED();
      static_for<0, 44>([&](auto ic) __attribute__((always_inline)) {
        constexpr int n = decltype(ic)::v, j = 3 - n / 11, g = n % 11;
        const v4u cur = hf[n % WIN];
        if (g == 0 && j < 3) {
          f32x4 nx[4];
#pragma unroll
          for (int i = 0; i < 4; ++i) nx[i] = mfma(w2f[0][i], cur, f32x4{0, 0, 0, 0});
          put(j + 1, ln);
          if (j == 0) lds_add(11, ln);
#pragma unroll
          for (int i = 0; i < 4; ++i) acc[i] = nx[i];
        } else {
#pragma unroll
          for (int i = 0; i < 4; ++i) acc[i] = mfma(w2f[g][i], cur, n == 0 ? f32x4{0, 0, 0, 0} : acc[i]);
        }
        SCHED();
        if (n + WIN < 44) ldh(n + WIN);
        if (n == GET5) { asm volatile("" : "+v"(acc[0]), "+v"(acc[1]), "+v"(acc[2]), "+v"(acc[3])); wait_lds0(cnt + 11, 4); get(ln); }
        SCHED();
      });
      STAMP_MID(12)
      f32x4 gv[4];
#pragma unroll
      for (int i = 0; i < 4; ++i) gv[i] = *(const f32x4*)(sll + 64 + 32 * (i >> 1) + 8 * (ln >> 4) + 4 * (i & 1));
      gate(gv);
      STAMP_MID(15)
#pragma unroll
      for (int ip = 0; ip < 2; ++ip) bst<AUX_OUT>(RSRC(out, 2048 * 1024), (ln & 15) * 1024 + (ln >> 4) * 16 + ip * 64, (p * 64 + w * 16) * 1024 + c * 128, res[ip]);
      if (b == 0 && w == 0 && ln == 0) st1(rs, 0, OFF_EPOCH, tag);
    }
    STAMP_END(w == 0 ? 2 : 8 + w, b, ws)
    return;
  }

  const int lane = fresh_lane(), m = lane & 15, q = lane >> 4, tid = w * 64 + lane;
  const unsigned l16 = lane * 16;
  const int wr = w >> 1, wc = w & 1;
  const int kg = lane >> 3, ng = lane & 7;
  auto job_store = [&]() {
    const int nkb = is1 ? NKB_X : NKB_H;
    const unsigned dst = (is1 ? OFF_W1 : OFF_W2) + ((jn0 >> 4) * nkb + (jk0 >> 5)) * 1024;
#pragma unroll
    for (int f = 0; f < 8; ++f) {
      const int ntl = f >> 1, kbl = f & 1;
      if (jk0 + kbl * 32 < (is1 ? D : F) && jn0 + (ntl >> 1) * 32 < (is1 ? F : D)) bst<SC1>(rs, l16, dst + (ntl * nkb + kbl) * 1024, R[f]);
    }
  };
  STAMP_MID(13)
  if (w >= 2) {
    if (jm < 192) {
      float a8[8] = {0.f, 0.f, 0.f, 0.f, 0.f, 0.f, 0.f, 0.f};
#pragma unroll
      for (int i = 0; i < 8; ++i) {
        const float c0 = (i & 1) ? bfhi(cvv[i >> 1]) : bflo(cvv[i >> 1]);
        const float sl = rbf(c0 / (1.f + __expf(-c0)));
#pragma unroll
        for (int u = 0; u < 4; ++u) { a8[2 * u] += sl * bflo(R[i][u]); a8[2 * u + 1] += sl * bfhi(R[i][u]); }
      }
      float* red = (float*)(shm + L_TOP + 16384);
      *(f32x4*)(red + ((w & 1) * 16 + (lane >> 2)) * 36 + (lane & 3) * 8) = f32x4{a8[0], a8[1], a8[2], a8[3]};
      *(f32x4*)(red + ((w & 1) * 16 + (lane >> 2)) * 36 + (lane & 3) * 8 + 4) = f32x4{a8[4], a8[5], a8[6], a8[7]};
      WAIT_L0();
      if (lds_inc(cnt + 5, lane) == 1u) {
        CFENCE();
        float s = 0.f;
#pragma unroll
        for (int i = 0; i < 16; ++i) s += red[((lane >> 5) * 16 + i) * 36 + (lane & 31)];
        float s2 = s;
        asm volatile("s_nop 1\n\tv_permlane32_swap_b32 %0, %1" : "+v"(s), "+v"(s2));
        s += s2;
        if (lane < 32) st1(rs, lane * 4, OFF_MOD + ((jm / 96) * 3072 + (jm % 96) * 32) * 4, asu(s));
      }
    }
  } else if (jb < 176) {
    char* lds = shm + L_TOP + w * 8192;
    const int a = ng & 3, qq = kg & 3;
    char* wp = lds + ((ng >> 2) * 4 + (kg >> 2)) * 1024 + (is1 ? (a >> 1) * 2048 + (qq * 16 + 8 * (a & 1)) * 16 : (qq * 16 + 4 * a) * 16);
    const int hi = is1 ? 64 : 2048;
#pragma unroll
    for (int jn = 0; jn < 8; ++jn) {
      const unsigned sel = (jn & 1) ? 0x07060302u : 0x05040100u;
      const int u = jn >> 1;
      v4u o;
      o.x = __builtin_amdgcn_perm(R[1][u], R[0][u], sel);
      o.y = __builtin_amdgcn_perm(R[3][u], R[2][u], sel);
      o.z = __builtin_amdgcn_perm(R[5][u], R[4][u], sel);
      o.w = __builtin_amdgcn_perm(R[7][u], R[6][u], sel);
      *(v4u*)(wp + (jn >> 2) * hi + (jn & 3) * 16) = o;
    }
    WAIT_L0();
    CFENCE();
#pragma unroll
    for (int f = 0; f < 8; ++f) R[f] = *(const v4u*)(lds + f * 1024 + l16);
    if (!LATE_ST) job_store();
  } else if (jb < 240) {
#pragma unroll
    for (int i = 0; i < 8; ++i) bst<SC1>(rs, l16, OFF_WD + ((w * 64 + jb - 176) * 8 + i) * 1024, R[i]);
  }
  SCHED();
  STAMP_MID(14)
  lds_inc(cnt + 12, lane);
  const int ft0 = (c < 6 ? 11 * c : 66 + 10 * (c - 6)) + 3 * w;
  const int nft = w < 3 ? 3 : (c < 6 ? 2 : 1);
  {
    const int xoff = q * 2048 + (wc * 64 + m) * 16;
    int wo[2];
#pragma unroll
    for (int kk = 0; kk < 2; ++kk) wo[kk] = 16384 + wr * 8192 + m * 128 + (((4 * kk + q) ^ ((m >> 1) & 7)) * 16);
    f32x4 acc[4][4], rsum = {0, 0, 0, 0};
#pragma unroll
    for (int i = 0; i < 4; ++i)
#pragma unroll
      for (int j = 0; j < 4; ++j) acc[i][j] = f32x4{0, 0, 0, 0};
    const v4u ones = {0x3f803f80u, 0x3f803f80u, 0x3f803f80u, 0x3f803f80u};
    v4u XA[4], WA[4], XB[4], WB[4];
    int sA = 0;
    auto rd = [&](int kk, v4u (&X)[4], v4u (&W)[4]) {
      W[0] = *(const v4u*)(shm + sA + wo[kk]);
      W[1] = *(const v4u*)(shm + sA + wo[kk] + 2048);
      W[2] = *(const v4u*)(shm + sA + wo[kk] + 6144 - wc * 2048);
      W[3] = *(const v4u*)(shm + sA + wo[kk] + 4096 + wc * 2048);
#pragma unroll
      for (int j = 0; j < 4; ++j) X[j] = *(const v4u*)(shm + sA + xoff + kk * 8192 + j * 256);
      if (kk) sA = (sA + STAGE) & (NS * STAGE - 1);
    };
    auto mm = [&](v4u (&X)[4], v4u (&W)[4]) {
#pragma unroll
      for (int j = 0; j < 4; ++j)
#pragma unroll
        for (int i = 0; i < 4; ++i) acc[i][j] = mfma(X[i], W[j], acc[i][j]);
      rsum = mfma(ones, W[3], rsum);
    };
    auto mix = [&]() {
#pragma unroll
      for (int i = 0; i < 8; ++i) {
        __builtin_amdgcn_sched_group_barrier(0x008, 1, 0);
        __builtin_amdgcn_sched_group_barrier(0x100, 1, 0);
      }
      __builtin_amdgcn_sched_group_barrier(0x008, 9, 0);
    };
    auto steps = [&](int n) {
#pragma unroll 1
      for (int t = 0; t < n; ++t) {
        SCHED();
        rd(1, XB, WB); mm(XA, WA); mix();
        SCHED();
        WAIT_L0();
        BAR();
        SCHED();
        rd(0, XA, WA); mm(XB, WB); mix();
      }
      SCHED();
    };
    if (PRIO) __builtin_amdgcn_s_setprio(1);
    BAR();
    STAMP_MID(0)
    rd(0, XA, WA);
    steps(2);
    WAIT_V0();
    if (lds_inc(cnt + 3, lane) == 3u) { if (lane < 8) flag_set(ws, rs, FLINE(LN_G2 + lane * 8 + (b >> 5)) + (b & 31) * 4, tag); }
    steps(7);
    unsigned g2 = ld1(rs, (lane >> 5) * FSTR + (lane & 31) * 4, FLINE(LN_G2 + xcd * 8 + w * 2));
    steps(2);
    if (__builtin_amdgcn_ballot_w64(g2 != tag) != 0ull) wait_words(rs, (lane >> 5) * FSTR + (lane & 31) * 4, FLINE(LN_G2 + xcd * 8 + w * 2), tag, true);
    CFENCE();
    steps(4);
    rd(1, XB, WB); mm(XA, WA); mix();
    SCHED();
    mm(XB, WB);
    STAMP_MID(1)
    f32x4 cav[4];
#pragma unroll
    for (int i = 0; i < 4; ++i) {
      const float* ml = (const float*)(shm + L_RAW + 6144) + wc * 64 + 32 * (i >> 1) + 8 * q + 4 * (i & 1);
      const f32x4 t = *(const f32x4*)ml + *(const f32x4*)(ml + 1792);
      const unsigned u0 = pk(t[0], t[1]), u1 = pk(t[2], t[3]);
      const unsigned z0 = pk(bflo(u0) + 1.f, bfhi(u0) + 1.f), z1 = pk(bflo(u1) + 1.f, bfhi(u1) + 1.f);
      cav[i] = f32x4{bflo(z0), bfhi(z0), bflo(z1), bfhi(z1)};
    }
#pragma unroll
    for (int jh = 0; jh < 2; ++jh) {
      const int gq = (2 * wr + jh + dq + 1) & 3;
      const float sg = (gq >> 1) ? -1.f : 1.f;
#pragma unroll
      for (int ip = 0; ip < 2; ++ip) {
        const f32x4 c0 = cav[2 * ip] * sg, c1 = cav[2 * ip + 1] * sg;
#pragma unroll
        for (int jl = 0; jl < 2; ++jl) {
          const int j = 2 * jh + jl;
          const int u = j < 2 ? j : (j == 3 ? wc : 1 - wc);
          const f32x4 a0 = acc[2 * ip][j] * c0, a1 = acc[2 * ip + 1][j] * c1;
          v4u o; o.x = pk(a0[0], a0[1]); o.y = pk(a0[2], a0[3]); o.z = pk(a1[0], a1[1]); o.w = pk(a1[2], a1[3]);
          const int tt = 4 * p + 2 * (gq & 1) + u, kb = (gq >> 1) * 16 + 4 * dq + 2 * wc + ip;
          bst<SC1>(rs, l16, OFF_PQ + ((tt * 32 + kb) * 2 + kh) * 1024, o);
        }
      }
    }
    if (wr == 1 && q < 2) st1(rs, lane * 4, OFF_RS + (p * 2 + (dq >> 1)) * 1024 + ((2 * (dq & 1) + wc) * 2 + kh) * 128, asu((dq >> 1) ? -rsum[0] : rsum[0]));
  }
  STAMP_MID(11)
  lds_inc(cnt + 6, lane);
  WAIT_V0();
  if (PRIV & 1) { if (lane < 8) flag_set(ws, rs, FLINE(LN_P1 + p * 8 + lane) + (c * 4 + w) * 4, tag); }
  else if (lane == 0) flag_set(ws, rs, FLINE(LN_F1 + p) + (c * 4 + w) * 4, tag);
  if (PRIO) __builtin_amdgcn_s_setprio(0);
  STAMP_MID(2)
  SCHED();
  if (LATE_ST) {
    if (w < 2) {
      if (jb < 176) job_store();
      WAIT_V0();
      if (lds_inc(cnt + 14, lane) == 1u) { if (lane < 8) flag_set(ws, rs, FLINE(LN_G3 + lane * 8 + (b >> 5)) + (b & 31) * 4, tag); }
    }
    if (LATE_TRIG) wait_lds(cnt + 0, 4); else __builtin_amdgcn_s_sleep(LATE_SLEEP);
    wait_words<16>(rs, (lane >> 5) * FSTR + (lane & 31) * 4, FLINE(LN_G3 + xcd * 8 + w * 2), tag, true);
    lds_inc(cnt + 7, lane);
    STAMP_MID(15)
  }
  constexpr int WK = 12;
  v4u w1f[WK][3];
  f32x4 accU[3];
  unsigned a1[3];
#pragma unroll
  for (int i = 0; i < 3; ++i) a1[i] = OFF_W1 + (ft0 + (i < nft ? i : 0)) * NKB_X * 1024;
  auto ldw1 = [&](int k, v4u (&d)[3]) {
    const unsigned ko = ((k + 2 * c) & 15) * 1024;
#pragma unroll
    for (int i = 0; i < 3; ++i) d[i] = bld<SC1>(rs, l16, a1[i] + ko);
  };
  auto upd = [&](int k, v4u (&d)[3], bool first) {
    const int kb = (k + 2 * c) & 15;
    const int ad = m == 8 ? L_MB + (512 + kb * 32 + q * 8) * 2 : (m == (kb >> 1) ? L_MB + (1024 + kb * 32 + q * 8) * 2 : L_ZERO);
    const v4u cf = *(const v4u*)(shm + ad);
#pragma unroll
    for (int i = 0; i < 3; ++i) accU[i] = mfma(cf, d[i], first ? f32x4{0, 0, 0, 0} : accU[i]);
  };
  auto cfad = [&](int k) {
    const int kb = (k + 2 * c) & 15;
    return m == 8 ? L_MB + (512 + kb * 32 + q * 8) * 2 : (m == (kb >> 1) ? L_MB + (1024 + kb * 32 + q * 8) * 2 : L_ZERO);
  };
  wait_lds(cnt + 7, LATE_ST ? 8 : 4);
#pragma unroll
  for (int k = 0; k < W1_B1; ++k) ldw1(k, w1f[k]);
  SCHED();
#pragma unroll
  for (int k = 0; k < W1_B1; ++k) upd(k, w1f[k], k == 0);
  SCHED();
  STAMP_MID(10)
  wait_lds(cnt + 8, 4);
#pragma unroll
  for (int k = W1_B1; k < WK; ++k) ldw1(k, w1f[k]);
#pragma unroll
  for (int k = WK; k < 16; ++k)
#pragma unroll
    for (int i = 0; i < 3; ++i) glds16<SC1>(ws + a1[i] + ((k + 2 * c) & 15) * 1024 + l16, (LAS char*)shm + L_W1X + w * 12288 + ((k - WK) * 3 + i) * 1024);
  WAIT_V0();
  char* w1x = shm + L_W1X + w * 12288 + l16;
#pragma unroll
  for (int k = W1_B1; k < WK; ++k) upd(k, w1f[k], false);
#pragma unroll
  for (int k = WK; k < 16; ++k) {
    v4u t[3];
#pragma unroll
    for (int i = 0; i < 3; ++i) t[i] = *(const v4u*)(w1x + ((k - WK) * 3 + i) * 1024);
    upd(k, t, false);
  }
  v4u uf[3];
#pragma unroll
  for (int i = 0; i < 3; ++i) {
    const unsigned h0 = pk(accU[i][0], accU[i][1]), h1 = pk(accU[i][2], accU[i][3]);
    uf[i] = v4u{h0, h1, pk(accU[i][0] - bflo(h0), accU[i][1] - bfhi(h0)), pk(accU[i][2] - bflo(h1), accU[i][3] - bfhi(h1))};
  }
  SCHED();
  STAMP_MID(9)
  f32x4 acc[3][4];
  v4u hb[2][4];
  auto rdh = [&](int k) {
#pragma unroll
    for (int j = 0; j < 4; ++j) hb[k & 1][j] = *(const v4u*)(shm + L_H2 + j * H2W + k * 1024 + l16);
  };
  if (PRIO) __builtin_amdgcn_s_setprio(2);
  BAR();
  SCHED();
  STAMP_MID(3)
  rdh(0);
#pragma unroll
  for (int k = 0; k < 16; ++k) {
    if (k == 3) { STAMP_MID(6) BAR(); STAMP_MID(7) }
    SCHED();
    rdh(k + 1);
    if (k >= 1 && k <= 16 - WK) {
#pragma unroll
      for (int i = 0; i < 3; ++i) w1f[k - 1][i] = *(const v4u*)(w1x + ((k - 1) * 3 + i) * 1024);
    }
    if (!P4MIX) SCHED();
#pragma unroll
    for (int j = 0; j < 4; ++j)
#pragma unroll
      for (int i = 0; i < 3; ++i) acc[i][j] = mfma(w1f[k % WK][i], hb[k & 1][j], k == 0 ? f32x4{0, 0, 0, 0} : acc[i][j]);
    if (P4MIX) {
#pragma unroll
      for (int i = 0; i < 7; ++i) { __builtin_amdgcn_sched_group_barrier(0x008, 1, 0); __builtin_amdgcn_sched_group_barrier(0x100, 1, 0); }
      __builtin_amdgcn_sched_group_barrier(0x008, 5, 0);
    }
    SCHED();
  }
  float rsd[4];
#pragma unroll
  for (int j = 0; j < 4; ++j) rsd[j] = ((const float*)(shm + L_RSTD))[j * 16 + m];
#pragma unroll
  for (int i = 0; i < 3; ++i)
#pragma unroll
    for (int j = 0; j < 4; ++j) acc[i][j] = mfma(uf[i], hb[0][j], acc[i][j]);
  STAMP_MID(4)
  const unsigned vo = (q & 1) * (NKB_H * 1024) + ((q >> 1) * 16 + m) * 16;
#pragma unroll
  for (int i = 0; i < 3; ++i) {
    if (i < nft) {
      const int ft = ft0 + i;
#pragma unroll
      for (int j = 0; j < 4; j += 2) {
        const f32x4 a = acc[i][j] * rsd[j], bb = acc[i][j + 1] * rsd[j + 1];
        unsigned a0 = pk(fmaxf(a[0], 0.f), fmaxf(a[1], 0.f)), a1v = pk(fmaxf(a[2], 0.f), fmaxf(a[3], 0.f));
        unsigned b0 = pk(fmaxf(bb[0], 0.f), fmaxf(bb[1], 0.f)), b1 = pk(fmaxf(bb[2], 0.f), fmaxf(bb[3], 0.f));
        asm volatile("s_nop 1\n\tv_permlane16_swap_b32 %0, %2\n\tv_permlane16_swap_b32 %1, %3" : "+v"(a0), "+v"(a1v), "+v"(b0), "+v"(b1));
        bst<SC1>(rs, vo, OFF_H1 + ((4 * p + j) * NKB_H + (ft >> 1)) * 1024 + (ft & 1) * 512, v4u{a0, a1v, b0, b1});
      }
    }
  }
  STAMP_MID(12)
  if (!F3_AD) lds_inc(cnt + 2, lane);
  WAIT_V0();
  if (PRIV & 4) { if (lane < 8) flag_set(ws, rs, FLINE(LN_P3 + p * 8 + lane) + (c * 4 + w) * 4, tag); }
  else if (lane == 0) flag_set(ws, rs, FLINE(LN_F3 + p * 4 + (c >> 1)) + ((c & 1) * 4 + w) * 4, tag);
  if (F3_AD) lds_inc(cnt + 2, lane);
  STAMP_MID(5)
  STAMP_END(w == 0 ? 1 : 5 + w, b, ws)
}

extern "C" void kernel_launch(void* const* d_in, const int* in_sizes, int n_in, void* d_out, int out_size, void* d_ws, size_t ws_size,
                              hipStream_t stream) {
  (void)hipFuncSetAttribute((const void*)fused_all, hipFuncAttributeMaxDynamicSharedMemorySize, K_LDS);
  fused_all<<<256, 512, K_LDS, stream>>>((const char*)d_in[0], (const char*)d_in[1], (const char*)d_in[2], (const char*)d_in[3], (const char*)d_in[4],
                                         (const char*)d_in[6], (const char*)d_in[7], (const char*)d_in[8], (const char*)d_in[9], (char*)d_ws, (char*)d_out);
}
```

```cpp
#include <hip/hip_runtime.h>
#include <hip/hip_bf16.h>
#include <stdint.h>

typedef unsigned v4u __attribute__((ext_vector_type(4)));
typedef unsigned v2u __attribute__((ext_vector_type(2)));
typedef float f32x4 __attribute__((ext_vector_type(4)));
typedef float f32x2 __attribute__((ext_vector_type(2)));
typedef short bf16x8 __attribute__((ext_vector_type(8)));
typedef __bf16 bf2 __attribute__((ext_vector_type(2)));
#define GAS __attribute__((address_space(1)))
#define LAS __attribute__((address_space(3)))
#define WAIT_V(n) asm volatile("s_waitcnt vmcnt(%0)" ::"n"(n) : "memory")
#define CFENCE() asm volatile("" ::: "memory")
#define WAIT_L0() do { CFENCE(); __builtin_amdgcn_s_waitcnt(0xC07F); } while (0)
#define WAIT_V0() do { WAIT_V(0); __builtin_amdgcn_s_waitcnt(0x0F70); CFENCE(); } while (0)
#define SCHED() __builtin_amdgcn_sched_barrier(0)
#define BAR() do { CFENCE(); __builtin_amdgcn_s_barrier(); CFENCE(); } while (0)
#define RSRC(p, n) __builtin_amdgcn_make_buffer_rsrc((void*)(p), 0, (n), 0x00020000)
constexpr int SC1 = 16;

constexpr int D = 512, F = 1376;
constexpr int NKB_X = 16, NKB_H = 43;
constexpr unsigned OFF_EPOCH = 0;
constexpr unsigned OFF_MOD  = 49152;
constexpr unsigned OFF_RS   = 131072;
constexpr unsigned OFF_ST   = 196608;
constexpr unsigned OFF_XNT  = 524288;
constexpr unsigned OFF_WD   = OFF_XNT + (2u << 20);
constexpr unsigned OFF_W1   = OFF_WD + (1u << 20);
constexpr unsigned OFF_W2   = OFF_W1 + 86 * 16 * 1024;
constexpr unsigned OFF_PQ   = OFF_W2 + 32 * 43 * 1024;
constexpr unsigned OFF_X1   = OFF_PQ + (8u << 20);
constexpr unsigned OFF_H1   = OFF_X1 + (2u << 20);
constexpr unsigned OFF_FL   = 24u << 20, FSTR = 256;
constexpr unsigned LN_G1 = 0, LN_G2 = 64, LN_G3 = 128;
constexpr unsigned LN_F1 = 192;
constexpr unsigned LN_F2 = 224, LN_F3 = 352;
constexpr unsigned LN_END = 480;
#define FLINE(l) (OFF_FL + (l) * FSTR)
constexpr unsigned WS_BYTES = FLINE(LN_END);
constexpr unsigned OOB = 0x4000000u;
constexpr int NS = 4, STAGE = 32768;
constexpr int L_TOP = NS * STAGE;
constexpr int L_RAW = L_TOP;
constexpr int L_MB = L_RAW + 14336;
constexpr int L_SL = L_MB + 3072;
constexpr int L_ZERO = L_TOP + 24576;
constexpr int L_CNT = L_ZERO + 64;
constexpr int L_RSTD = L_CNT + 64;
constexpr int L_DUMMY = L_RSTD + 256;
constexpr int K_LDS = L_DUMMY + 256;
constexpr int L_LN = 3 * STAGE;
constexpr int L_H2 = 0, H2W = 17 * 1024;
constexpr int L_W1X = 4 * H2W;
enum { C_G3GO = 0, C_G1 = 1, C_P4 = 2, C_JOB = 3, C_F1 = 4, C_MOD = 5, C_RING = 6, C_W1 = 7, C_F2 = 8, C_COR = 9, C_RED3 = 10, C_RED5 = 11, C_VEC = 13, C_LATE = 14 };
constexpr unsigned SPIN_CAP = 1u << 18;
constexpr int CODE_KB = 35;

template <int I> struct IC { static constexpr int v = I; };
template <int B, int E, class Fn> __device__ __forceinline__ void static_for(Fn&& f) { if constexpr (B < E) { f(IC<B>{}); static_for<B + 1, E>(f); } }
__device__ __forceinline__ float asf(unsigned u) { return __builtin_bit_cast(float, u); }
__device__ __forceinline__ unsigned asu(float f) { return __builtin_bit_cast(unsigned, f); }
__device__ __forceinline__ float bflo(unsigned u) { return asf(u << 16); }
__device__ __forceinline__ float bfhi(unsigned u) { return asf(u & 0xffff0000u); }
__device__ __forceinline__ unsigned pk(float lo, float hi) {
  f32x2 v = {lo, hi};
  return __builtin_bit_cast(unsigned, __builtin_convertvector(v, bf2));
}
__device__ __forceinline__ float rbf(float x) { return bflo(pk(x, x)); }
__device__ __forceinline__ f32x4 mfma(v4u a, v4u b, f32x4 c) {
  return __builtin_amdgcn_mfma_f32_16x16x32_bf16(__builtin_bit_cast(bf16x8, a), __builtin_bit_cast(bf16x8, b), c, 0, 0, 0);
}
template <int CTRL> __device__ __forceinline__ float dpp_add(float x) {
  int y = __builtin_amdgcn_update_dpp(0, __builtin_bit_cast(int, x), CTRL, 0xf, 0xf, false);
  return x + __builtin_bit_cast(float, y);
}
__device__ __forceinline__ float wave_sum(float x) {
  x = dpp_add<0x111>(x); x = dpp_add<0x112>(x); x = dpp_add<0x114>(x); x = dpp_add<0x118>(x);
  x = dpp_add<0x142>(x); x = dpp_add<0x143>(x);
  return __builtin_bit_cast(float, __builtin_amdgcn_readlane(__builtin_bit_cast(int, x), 63));
}
__device__ __forceinline__ float half_sum(float x) {
  float y = x;
  asm volatile("s_nop 1\n\tv_permlane32_swap_b32 %0, %1" : "+v"(x), "+v"(y));
  return x + y;
}
__device__ __forceinline__ float quad16_sum(float x) {
  float y = x;
  asm volatile("s_nop 1\n\tv_permlane16_swap_b32 %0, %1" : "+v"(x), "+v"(y));
  return half_sum(x + y);
}
__device__ __forceinline__ int fresh_lane() { int l; asm volatile("v_mbcnt_lo_u32_b32 %0, -1, 0\n\tv_mbcnt_hi_u32_b32 %0, -1, %0" : "=v"(l)); return l; }
template <int AUX> __device__ __forceinline__ v4u bld(__amdgpu_buffer_rsrc_t rs, unsigned voff, unsigned soff) { return __builtin_amdgcn_raw_buffer_load_b128(rs, voff, soff, AUX); }
template <int AUX> __device__ __forceinline__ void bst(__amdgpu_buffer_rsrc_t rs, unsigned voff, unsigned soff, v4u v) { __builtin_amdgcn_raw_buffer_store_b128(v, rs, voff + soff, 0, AUX); }
__device__ __forceinline__ void st1(__amdgpu_buffer_rsrc_t rs, unsigned voff, unsigned soff, unsigned v) { __builtin_amdgcn_raw_buffer_store_b32(v, rs, voff, soff, SC1); }
__device__ __forceinline__ unsigned ld1(__amdgpu_buffer_rsrc_t rs, unsigned voff, unsigned soff) { return __builtin_amdgcn_raw_buffer_load_b32(rs, voff, soff, SC1); }
template <int AUX> __device__ __forceinline__ void glds16(const char* g, LAS char* l) {
  __builtin_amdgcn_global_load_lds((const GAS unsigned*)g, (LAS unsigned*)l, 16, 0, AUX);
}
__device__ __forceinline__ void flag_set(char* ws, unsigned off, unsigned v) {
  asm volatile("global_atomic_swap %0, %1, %2 sc1" :: "v"(off), "v"(v), "s"(ws) : "memory");
}
template <int SLP> __device__ __forceinline__ void wait_words(__amdgpu_buffer_rsrc_t rs, unsigned voff, unsigned soff, unsigned tag) {
#pragma unroll 1
  for (unsigned spins = 0; spins < SPIN_CAP; ++spins) {
    CFENCE();
    const unsigned v = ld1(rs, voff, soff);
    if (__builtin_amdgcn_ballot_w64(v != tag) == 0ull) break;
    __builtin_amdgcn_s_sleep(SLP);
  }
  __builtin_amdgcn_fence(__ATOMIC_ACQUIRE, "wavefront");
  CFENCE();
  SCHED();
}
template <bool SLEEP> __device__ __forceinline__ void wait_lds(int idx, unsigned n) {
#pragma unroll 1
  for (unsigned spins = 0; spins < SPIN_CAP * 64; ++spins) {
    unsigned v;
    asm volatile("ds_read_b32 %0, %1\n\ts_waitcnt lgkmcnt(0)" : "=v"(v) : "v"(L_CNT + idx * 4) : "memory");
    if (__builtin_amdgcn_readfirstlane(v) >= n) break;
    if (SLEEP) __builtin_amdgcn_s_sleep(1);
  }
  SCHED();
}
__device__ __forceinline__ void lds_add(int idx) {
  unsigned long long ex;
  asm volatile("s_mov_b64 %0, exec\n\ts_mov_b64 exec, 1\n\tds_add_u32 %1, %2\n\ts_mov_b64 exec, %0" : "=&s"(ex) : "v"(L_CNT + idx * 4), "v"(1u) : "memory");
}
__device__ __forceinline__ unsigned lds_inc(int idx) {
  unsigned long long ex; unsigned old;
  asm volatile("s_mov_b64 %0, exec\n\ts_mov_b64 exec, 1\n\tds_add_rtn_u32 %1, %2, %3\n\ts_waitcnt lgkmcnt(0)\n\ts_mov_b64 exec, %0"
               : "=&s"(ex), "=&v"(old) : "v"(L_CNT + idx * 4), "v"(1u) : "memory");
  return __builtin_amdgcn_readfirstlane(old);
}

__global__ __launch_bounds__(512, 2) void fused_all(const char* __restrict__ x, const char* __restrict__ wsr, const char* __restrict__ wsi,
                                                    const char* __restrict__ wdr, const char* __restrict__ wdi,
                                                    const char* __restrict__ w1, const char* __restrict__ w2, const char* __restrict__ cvec,
                                                    const char* __restrict__ wmod, char* __restrict__ ws, char* __restrict__ out) {
  extern __shared__ __attribute__((aligned(16))) char shm[];
  const int b = blockIdx.x, xcd = b & 7, idx = b >> 3, pl = idx >> 3, p = xcd * 4 + pl, c = idx & 7, dq = c & 3, kh = c >> 2;
  const int w8 = __builtin_amdgcn_readfirstlane(threadIdx.x >> 6), w = w8 & 3;
  const __amdgpu_buffer_rsrc_t rs = RSRC(ws, WS_BYTES);
  const float* sll = (const float*)(shm + L_SL);

  unsigned long long pc0;
  asm volatile("s_getpc_b64 %0" : "=s"(pc0));
  auto code_warm = [&](int piece, int lane, int ldsoff) {
    const unsigned sl = piece * 64 + lane;
    if (sl < CODE_KB * 8) __builtin_amdgcn_global_load_lds((const GAS unsigned*)((pc0 & ~127ull) + (unsigned long long)sl * 128), (LAS unsigned*)((LAS char*)shm + ldsoff), 4, 0, 0);
  };
  if (idx == 0 && w8 < 5) code_warm(w8, threadIdx.x & 63, L_LN + 8192 + w8 * 256);
  SCHED();
  v4u xq; unsigned tq;
  asm volatile("global_load_dwordx4 %0, %2, %3\n\tglobal_load_dword %1, %4, %5 sc1"
               : "=&v"(xq), "=&v"(tq) : "v"(b * 8192 + threadIdx.x * 16), "s"(x), "v"(OFF_EPOCH), "s"(ws) : "memory");
  SCHED();
  v4u R[8], cvv;
  const int jb = c * 32 + pl * 8 + xcd;
  const int jm = ((c * 2 + (pl >> 1)) * 8 + xcd) * 2 + (pl & 1);
  const bool is1 = w == 0;
  const int jk0 = (is1 ? (jb & 7) : (jb >> 3)) * 64, jn0 = (is1 ? (jb >> 3) : (jb & 7)) * 64;
  {
    const int lane = threadIdx.x & 63, m = lane & 15, q = lane >> 4, kg = lane >> 3, ng = lane & 7;
    const char* base; unsigned off, ld, len;
    if (w8 >= 4 || (w >= 2 ? jm >= 192 : jb >= 240)) { base = cvec; ld = 0; off = 0; len = 0; }
    else if (w >= 2) { base = wmod; ld = 6144; len = 512 * 6144; off = ((jm / 96) * 256 + (w & 1) * 128 + (lane >> 2) * 8) * 6144 + (jm % 96) * 64 + (lane & 3) * 16; }
    else if (jb < 176) {
      base = is1 ? w1 : w2; ld = is1 ? F * 2 : D * 2; len = D * F * 2;
      const bool ok = (jk0 + kg * 8 < (is1 ? D : F)) && (jn0 + ng * 8 < (is1 ? F : D));
      off = ok ? (jk0 + kg * 8) * ld + (jn0 + ng * 8) * 2 : OOB;
    } else {
      const int j3 = w * 64 + jb - 176, et = j3 >> 2, kb0 = (j3 & 3) * 8;
      base = kb0 < 16 ? wdr : wdi; ld = 64; len = D * D * 2;
      off = (32 * (et >> 1) + 8 * (m >> 2) + 4 * (et & 1) + (m & 3)) * 1024 + (kb0 & 15) * 64 + q * 16;
    }
    ld = __builtin_amdgcn_readfirstlane(ld);
    len = __builtin_amdgcn_readfirstlane(len);
    const __amdgpu_buffer_rsrc_t rj = RSRC(base, len);
#pragma unroll
    for (int k = 0; k < 8; ++k) R[k] = bld<0>(rj, off, k * ld);
    cvv = bld<0>(RSRC(cvec, 1024), ((w & 1) * 128 + (lane >> 2) * 8) * 2, (jm / 96 & 1) * 512);
    SCHED();
  }
  asm volatile("s_waitcnt vmcnt(9)" : "+v"(xq), "+v"(tq) :: "memory");
  unsigned tag;
  {
    const int tid = threadIdx.x, lane = tid & 63;
    float v[8];
#pragma unroll
    for (int u = 0; u < 4; ++u) { v[2 * u] = bflo(xq[u]); v[2 * u + 1] = bfhi(xq[u]); }
    const float mean = wave_sum(((v[0] + v[1]) + (v[2] + v[3])) + ((v[4] + v[5]) + (v[6] + v[7]))) * (1.f / D);
    float ss = 0.f;
#pragma unroll
    for (int j = 0; j < 8; ++j) { const float t = v[j] - mean; ss += t * t; }
    const float var = wave_sum(ss) * (1.f / D);
    const float mb = rbf(mean);
    const float rsd = 1.f / rbf(__builtin_sqrtf(rbf(rbf(var) + 1e-5f)));
    v4u o;
#pragma unroll
    for (int u = 0; u < 4; ++u) {
      const unsigned d2 = pk(v[2 * u] - mb, v[2 * u + 1] - mb);
      o[u] = pk(bflo(d2) * rsd, bfhi(d2) * rsd);
    }
    *(v4u*)(shm + L_LN + tid * 16) = o;
    if (tid < 32) ((unsigned*)(shm + L_ZERO))[tid] = 0u;
    WAIT_L0();
    BAR();
    const int d = (tid & ~31) + 8 * ((tid >> 2) & 3) + 4 * ((tid >> 4) & 1) + (tid & 3);
    const unsigned short* sp = (const unsigned short*)(shm + L_LN) + d;
    v4u t;
#pragma unroll
    for (int u = 0; u < 4; ++u) t[u] = (unsigned)sp[(2 * u) * 512] | ((unsigned)sp[(2 * u + 1) * 512] << 16);
    tag = __builtin_amdgcn_readfirstlane(tq) * 0x9E3779B1u + 0x7F4A7C15u;
    bst<SC1>(rs, tid * 16, OFF_XNT + b * 8192, t);
    WAIT_V0();
    BAR();
    if (w8 == 0) { if (lane < 8) flag_set(ws, FLINE(LN_G1 + lane * 8 + (b >> 5)) + (b & 31) * 4, tag); }
    else __builtin_amdgcn_s_sleep(1);
  }

  if (w8 >= 4) {
    const int lane = fresh_lane(), m = lane & 15, q = lane >> 4;
    const unsigned l16 = lane * 16;
    v4u wd[8][4], res[2];
    {
      const int gq = (w + dq + 1) & 3;
      const char* wbase = (gq < 2 ? wsr : wsi) + (size_t)(p * 64 + (gq & 1) * 32 + (lane >> 3)) * 4096 + kh * 2048;
      const int sl = lane & 7, h = lane >> 4;
      const char* wsrc0 = wbase + ((sl ^ h) * 16);
      const char* wsrc1 = wbase + ((sl ^ (4 + h)) * 16) + 8 * 4096;
      const char* xsrc = ws + OFF_XNT + ((size_t)(kh * 128 + 2 * w) * 512 + dq * 128 + lane) * 16;
      auto issW = [&](int st) {
        LAS char* dst = (LAS char*)shm + (st & 3) * STAGE + w * 4096 + 16384;
#pragma unroll
        for (int i = 0; i < 4; ++i) glds16<2>(((i & 1) ? wsrc1 : wsrc0) + (size_t)(i >> 1) * 16 * 4096 + st * 128, dst + i * 1024);
      };
      auto issX = [&](int st) {
        LAS char* dst = (LAS char*)shm + (st & 3) * STAGE + w * 4096;
#pragma unroll
        for (int i = 0; i < 4; ++i) glds16<SC1>(xsrc + (size_t)st * 65536 + (i >> 1) * 8192 + (i & 1) * 1024, dst + i * 1024);
      };
      issW(0); issW(1); issW(2); issW(3);
#pragma unroll
      for (int ip = 0; ip < 2; ++ip) res[ip] = bld<0>(RSRC(x, 2048 * 1024), m * 1024 + q * 16 + ip * 64, (p * 64 + w * 16) * 1024 + c * 128);
      SCHED();
      if (w < 2) { wait_words<2>(rs, (lane >> 5) * FSTR + (lane & 31) * 4, FLINE(LN_G1 + xcd * 8 + kh * 4 + w * 2), tag); lds_add(C_G1); }
      wait_lds<true>(C_G1, 2);
      issX(0); issX(1); issX(2); issX(3);
      WAIT_V(12); BAR();
      WAIT_V(8); BAR();
      issX(4);
      WAIT_V(8); BAR();
      issW(4); issW(5);
      WAIT_V(12); BAR();
      issX(5); issX(6);
#pragma unroll 1
      for (int t = 3; t < 11; t += 2) {
        WAIT_V(12); BAR();
        issW(t + 3); issW(t + 4);
        WAIT_V(12); BAR();
        issX(t + 4); issX(t + 5);
      }
      WAIT_V(12); BAR();
      issW(14); issW(15);
      const unsigned a0 = OFF_WD + ((4 * c) * 32 + 8 * w) * 1024;
      auto ldwd = [&](int f0, int f1) {
        SCHED();
#pragma unroll
        for (int f = f0; f < f1; ++f) wd[f >> 2][f & 3] = bld<SC1>(rs, l16, a0 + ((f & 3) * 32 + (f >> 2)) * 1024);
        SCHED();
      };
      WAIT_V(12); BAR();
      issX(15);
      {
        const char* mp = ws + OFF_MOD + (w & 1) * 12288;
        LAS char* dst = (LAS char*)shm + L_RAW + (w & 1) * 7168;
        const unsigned sl_off = lane < 32 ? 2048 + dq * 512 + l16 : (lane < 48 ? 4096 + c * 256 + l16 - 512 : 10240 + c * 256 + l16 - 768);
#pragma unroll
        for (int i = 0; i < 4; ++i) {
          const int pc = (w >> 1) * 4 + i;
          const unsigned so = pc < 2 ? pc * 1024 + l16 : (pc < 6 ? 4096 + pc * 1024 + l16 : sl_off);
          glds16<SC1>(mp + so, dst + (pc < 6 ? pc : 6) * 1024);
        }
      }
      WAIT_V(12); BAR();
      ldwd(0, 12);
      WAIT_V(12); BAR();
      ldwd(12, 24);
      {
        const int tid = w * 64 + lane;
        f32x2 pa[3], pb[3];
        float sa1, sb1;
        asm volatile("ds_read_b64 %0, %8\n\tds_read_b64 %1, %8 offset:2048\n\tds_read_b64 %2, %8 offset:4096\n\t"
                     "ds_read_b64 %3, %8 offset:7168\n\tds_read_b64 %4, %8 offset:9216\n\tds_read_b64 %5, %8 offset:11264\n\t"
                     "ds_read_b32 %6, %9\n\tds_read_b32 %7, %9 offset:7168\n\ts_waitcnt lgkmcnt(0)"
                     : "=&v"(pa[0]), "=&v"(pa[1]), "=&v"(pa[2]), "=&v"(pb[0]), "=&v"(pb[1]), "=&v"(pb[2]), "=&v"(sa1), "=&v"(sb1)
                     : "v"(L_RAW + tid * 8), "v"(tid < 128 ? L_RAW + 6656 + tid * 4 : L_RAW + 4096 + c * 256 + (tid & 63) * 4) : "memory");
        unsigned z[3];
#pragma unroll
        for (int i = 0; i < 3; ++i) {
          z[i] = pk(pa[i][0] + pb[i][0], pa[i][1] + pb[i][1]);
          if (i == 2) z[i] = pk(bflo(z[i]) + 1.f, bfhi(z[i]) + 1.f);
        }
        float sv = rbf(sa1 + sb1);
        if (tid >= 128) sv = rbf(sv + 1.f);
        asm volatile("ds_write_b32 %0, %1\n\tds_write_b32 %0, %2 offset:1024\n\tds_write_b32 %0, %3 offset:2048\n\tds_write_b32 %0, %4 offset:3072"
                     :: "v"(L_MB + tid * 4), "v"(z[0]), "v"(z[1]), "v"(z[2]), "v"(sv) : "memory");
        lds_add(C_VEC);
      }
      wait_lds<true>(C_RING, 4);
      ldwd(24, 32);
      wait_lds<true>(C_VEC, 4);
    }
    SCHED();
    f32x4 acc[4], oth[4];
    f32x4* red = (f32x4*)(shm + L_H2);
    auto put = [&](int j, int ln) {
#pragma unroll
      for (int i = 0; i < 4; ++i) red[((j + w) & 3) * (H2W / 16) + ((j - 1) * 4 + i) * 64 + ln] = acc[i];
    };
    auto get = [&](int ln) {
#pragma unroll
      for (int i = 0; i < 4; ++i)
        oth[i] = (red[w * (H2W / 16) + i * 64 + ln] + red[w * (H2W / 16) + (4 + i) * 64 + ln]) + red[w * (H2W / 16) + (8 + i) * 64 + ln];
    };
    {
      f32x4 accS[4];
      v4u sf[8];
#pragma unroll
      for (int k = 0; k < 8; ++k) sf[k] = *(const v4u*)(shm + L_MB + ((8 * (w & 1) + k) * 32 + q * 8) * 2);
      SCHED();
#pragma unroll
      for (int k = 0; k < 8; ++k)
#pragma unroll
        for (int i = 0; i < 4; ++i) accS[i] = mfma(wd[k][i], sf[k], k == 0 ? f32x4{0, 0, 0, 0} : accS[i]);
      SCHED();
      if (w == 0) { wait_words<2>(rs, (lane & 31) * 4, FLINE(LN_F1 + p), tag); lds_add(C_F1); }
      else wait_lds<true>(C_F1, 1);
      constexpr int WIN = 12;
      f32x2 rr[4];
      v4u pq[WIN];
      unsigned b0[4];
#pragma unroll
      for (int j = 0; j < 4; ++j) b0[j] = OFF_PQ + ((4 * p + ((j + w) & 3)) * 64 + 16 * w) * 1024;
      auto ldr = [&](int j) {
#pragma unroll
        for (int h = 0; h < 2; ++h) rr[j][h] = asf(ld1(rs, m * 4 + h * 128, OFF_RS + (p * 2 + (w >> 1)) * 1024 + ((j + w) & 3) * 256));
      };
      auto ldp = [&](int n) { pq[n % WIN] = bld<SC1>(rs, l16, b0[3 - (n >> 4)] + (n & 15) * 1024); };
      ldr(3);
      SCHED();
#pragma unroll
      for (int n = 0; n < WIN; ++n) ldp(n);
      SCHED();
      ldr(2); ldr(1); ldr(0);
      SCHED();
      static_for<0, 64>([&](auto ic) __attribute__((always_inline)) {
        constexpr int n = decltype(ic)::v, j = 3 - (n >> 4), g = n & 15;
        const v4u cur = pq[n % WIN];
        if (g == 0 && j < 3) {
          f32x4 nx[4];
#pragma unroll
          for (int i = 0; i < 4; ++i) nx[i] = mfma(wd[0][i], cur, accS[i] * (rr[j][0] + rr[j][1]));
          put(j + 1, lane);
          if (j == 0) lds_add(C_RED3);
#pragma unroll
          for (int i = 0; i < 4; ++i) acc[i] = nx[i];
        } else {
#pragma unroll
          for (int i = 0; i < 4; ++i) acc[i] = mfma(wd[g >> 1][i], cur, n == 0 ? accS[i] * (rr[j][0] + rr[j][1]) : acc[i]);
        }
        SCHED();
        if (n + WIN < 64) ldp(n + WIN);
        if (n + WIN == 63) lds_add(C_W1);
        if (n == 40) lds_add(C_G3GO);
        if (n == 58) { asm volatile("" : "+v"(acc[0]), "+v"(acc[1]), "+v"(acc[2]), "+v"(acc[3])); wait_lds<false>(C_RED3, 4); get(lane); }
        SCHED();
      });
    }
    auto gate = [&](const f32x4 (&gv)[4]) {
#pragma unroll
      for (int i = 0; i < 4; ++i) {
        const f32x4 t = (acc[i] + oth[i]) * gv[i];
        const unsigned r0 = res[i >> 1][2 * (i & 1)], r1 = res[i >> 1][2 * (i & 1) + 1];
        res[i >> 1][2 * (i & 1)] = pk(bflo(r0) + t[0], bfhi(r0) + t[1]);
        res[i >> 1][2 * (i & 1) + 1] = pk(bflo(r1) + t[2], bfhi(r1) + t[3]);
      }
    };
    const unsigned xb = OFF_X1 + (4 * p + w) * NKB_X * 1024, sb0 = OFF_ST + (p * 4 + w) * 1024;
    {
      f32x4 cv[4], gv[4];
#pragma unroll
      for (int i = 0; i < 4; ++i) { gv[i] = *(const f32x4*)(sll + 32 * (i >> 1) + 8 * q + 4 * (i & 1)); cv[i] = *(const f32x4*)(sll + 128 + 32 * (i >> 1) + 8 * q + 4 * (i & 1)); }
      gate(gv);
      float v[16];
#pragma unroll
      for (int u = 0; u < 8; ++u) { v[2 * u] = bflo(res[u >> 2][u & 3]); v[2 * u + 1] = bfhi(res[u >> 2][u & 3]); }
      float s = 0.f;
#pragma unroll
      for (int u = 0; u < 16; u += 4) s += (v[u] + v[u + 1]) + (v[u + 2] + v[u + 3]);
      const float mc = quad16_sum(s) * (1.f / 64);
#pragma unroll
      for (int u = 0; u < 16; ++u) v[u] -= mc;
#pragma unroll
      for (int ip = 0; ip < 2; ++ip) {
        v4u o;
#pragma unroll
        for (int u = 0; u < 4; ++u) o[u] = pk(v[8 * ip + 2 * u] * cv[2 * ip + (u >> 1)][(2 * u) & 3], v[8 * ip + 2 * u + 1] * cv[2 * ip + (u >> 1)][(2 * u + 1) & 3]);
        bst<SC1>(rs, l16, xb + (2 * c + ip) * 1024, o);
        *(v4u*)(shm + L_H2 + w * H2W + ip * 1024 + l16) = o;
      }
      float ss = 0.f, s2 = 0.f;
#pragma unroll
      for (int u = 0; u < 16; u += 2) { ss += v[u] * v[u]; s2 += v[u + 1] * v[u + 1]; }
      ss = quad16_sum(ss + s2);
      if (q == 0) __builtin_amdgcn_raw_buffer_store_b64(v2u{asu(mc), asu(ss)}, rs, m * 8, sb0 + c * 128, SC1);
      WAIT_V0();
      if (lane == 0) flag_set(ws, FLINE(LN_F2 + p * 4 + w) + c * 4, tag);
      lds_add(C_F2);
    }
    asm volatile("" : "+v"(res[0]), "+v"(res[1]));
    SCHED();
    __builtin_amdgcn_s_sleep(10);
    wait_words<2>(rs, (lane & 7) * 4, FLINE(LN_F2 + p * 4 + w), tag);
    {
      v4u X[14];
      v2u st[8];
#pragma unroll
      for (int k = 0; k < 14; ++k) X[k] = bld<SC1>(rs, l16, xb + ((2 * c + 2 + k) & 15) * 1024);
#pragma unroll
      for (int i = 0; i < 8; ++i) st[i] = __builtin_amdgcn_raw_buffer_load_b64(rs, m * 8 + i * 128, sb0, SC1);
      SCHED();
#pragma unroll
      for (int k = 0; k < 14; ++k) {
        *(v4u*)(shm + L_H2 + w * H2W + (2 + k) * 1024 + l16) = X[k];
        if (k == 1) { WAIT_L0(); BAR(); SCHED(); }
      }
      float mu[8], mean = 0.f, m2 = 0.f;
#pragma unroll
      for (int i = 0; i < 8; ++i) { mu[i] = asf(st[i].x); mean += mu[i]; m2 += asf(st[i].y); }
      mean *= 0.125f;
      float dv = 0.f;
#pragma unroll
      for (int i = 0; i < 8; ++i) { const float d0 = mu[i] - mean; dv += d0 * d0; }
      m2 += 64.f * dv;
      const float den = rbf(__builtin_sqrtf(rbf(rbf(m2 * (1.f / D)) + 1e-5f)));
      const float mb = rbf(mean);
      unsigned d0 = q == 0 ? pk(mu[0] - mb, mu[1] - mb) : pk(mu[4] - mb, mu[5] - mb);
      unsigned d1 = q == 0 ? pk(mu[2] - mb, mu[3] - mb) : pk(mu[6] - mb, mu[7] - mb);
      if (q >= 2) { d0 = q == 2 ? pk(den, 0.f) : 0u; d1 = 0u; }
      *(v4u*)(shm + L_H2 + w * H2W + 16 * 1024 + l16) = v4u{d0, d1, d0, d1};
      if (q == 0) ((float*)(shm + L_RSTD))[w * 16 + m] = 1.f / den;
      WAIT_L0(); BAR(); SCHED();
    }
    {
      constexpr int WIN = 8;
      v4u w2f[11][4];
      const unsigned a0 = OFF_W2 + ((4 * c) * NKB_H + 11 * w) * 1024;
      const unsigned last = w == 3 ? OOB : 10 * 1024;
#pragma unroll
      for (int k = 0; k < 11; ++k)
#pragma unroll
        for (int i = 0; i < 4; ++i) w2f[k][i] = bld<SC1>(rs, l16, a0 + i * NKB_H * 1024 + (k < 10 ? k * 1024 : last));
      SCHED();
      wait_lds<true>(C_P4, 4);
      const int ln = fresh_lane();
      __builtin_amdgcn_s_sleep(14);
      wait_words<2>(rs, (ln & 7) * 4, FLINE(LN_F3 + p * 4 + w), tag);
      v4u hf[WIN];
      unsigned b0[4];
#pragma unroll
      for (int j = 0; j < 4; ++j) b0[j] = OFF_H1 + ((4 * p + ((j + w) & 3)) * NKB_H + 11 * w) * 1024;
      auto ldh = [&](int n) { hf[n % WIN] = bld<SC1>(rs, ln * 16, b0[3 - n / 11] + (n % 11 < 10 ? (n % 11) * 1024 : last)); };
#pragma unroll
      for (int n = 0; n < WIN; ++n) ldh(n);
      SCHED();
      static_for<0, 44>([&](auto ic) __attribute__((always_inline)) {
        constexpr int n = decltype(ic)::v, j = 3 - n / 11, g = n % 11;
        const v4u cur = hf[n % WIN];
        if (g == 0 && j < 3) {
          f32x4 nx[4];
#pragma unroll
          for (int i = 0; i < 4; ++i) nx[i] = mfma(w2f[0][i], cur, f32x4{0, 0, 0, 0});
          put(j + 1, ln);
          if (j == 0) lds_add(C_RED5);
#pragma unroll
          for (int i = 0; i < 4; ++i) acc[i] = nx[i];
        } else {
#pragma unroll
          for (int i = 0; i < 4; ++i) acc[i] = mfma(w2f[g][i], cur, n == 0 ? f32x4{0, 0, 0, 0} : acc[i]);
        }
        SCHED();
        if (n + WIN < 44) ldh(n + WIN);
        if (n == 40) { asm volatile("" : "+v"(acc[0]), "+v"(acc[1]), "+v"(acc[2]), "+v"(acc[3])); wait_lds<false>(C_RED5, 4); get(ln); }
        SCHED();
      });
      f32x4 gv[4];
#pragma unroll
      for (int i = 0; i < 4; ++i) gv[i] = *(const f32x4*)(sll + 64 + 32 * (i >> 1) + 8 * (ln >> 4) + 4 * (i & 1));
      gate(gv);
#pragma unroll
      for (int ip = 0; ip < 2; ++ip) bst<0>(RSRC(out, 2048 * 1024), (ln & 15) * 1024 + (ln >> 4) * 16 + ip * 64, (p * 64 + w * 16) * 1024 + c * 128, res[ip]);
      if (b == 0 && w == 0 && ln == 0) st1(rs, 0, OFF_EPOCH, tag);
    }
    return;
  }

  const int lane = fresh_lane(), m = lane & 15, q = lane >> 4;
  const unsigned l16 = lane * 16;
  const int wr = w >> 1, wc = w & 1;
  if (w >= 2) {
    if (jm < 192) {
      float a8[8] = {0.f, 0.f, 0.f, 0.f, 0.f, 0.f, 0.f, 0.f};
#pragma unroll
      for (int i = 0; i < 8; ++i) {
        const float c0 = (i & 1) ? bfhi(cvv[i >> 1]) : bflo(cvv[i >> 1]);
        const float sl = rbf(c0 / (1.f + __expf(-c0)));
#pragma unroll
        for (int u = 0; u < 4; ++u) { a8[2 * u] += sl * bflo(R[i][u]); a8[2 * u + 1] += sl * bfhi(R[i][u]); }
      }
      float* red = (float*)(shm + L_TOP + 16384);
      *(f32x4*)(red + ((w & 1) * 16 + (lane >> 2)) * 36 + (lane & 3) * 8) = f32x4{a8[0], a8[1], a8[2], a8[3]};
      *(f32x4*)(red + ((w & 1) * 16 + (lane >> 2)) * 36 + (lane & 3) * 8 + 4) = f32x4{a8[4], a8[5], a8[6], a8[7]};
      if (lds_inc(C_MOD) == 1u) {
        float s = 0.f;
#pragma unroll
        for (int i = 0; i < 16; ++i) s += red[((lane >> 5) * 16 + i) * 36 + (lane & 31)];
        s = half_sum(s);
        if (lane < 32) st1(rs, lane * 4, OFF_MOD + ((jm / 96) * 3072 + (jm % 96) * 32) * 4, asu(s));
      }
    }
  } else if (jb < 176) {
    const int kg = lane >> 3, ng = lane & 7;
    char* lds = shm + L_TOP + w * 8192;
    const int a = ng & 3, qq = kg & 3;
    char* wp = lds + ((ng >> 2) * 4 + (kg >> 2)) * 1024 + (is1 ? (a >> 1) * 2048 + (qq * 16 + 8 * (a & 1)) * 16 : (qq * 16 + 4 * a) * 16);
    const int hi = is1 ? 64 : 2048;
#pragma unroll
    for (int jn = 0; jn < 8; ++jn) {
      const unsigned sel = (jn & 1) ? 0x07060302u : 0x05040100u;
      const int u = jn >> 1;
      v4u o;
      o.x = __builtin_amdgcn_perm(R[1][u], R[0][u], sel);
      o.y = __builtin_amdgcn_perm(R[3][u], R[2][u], sel);
      o.z = __builtin_amdgcn_perm(R[5][u], R[4][u], sel);
      o.w = __builtin_amdgcn_perm(R[7][u], R[6][u], sel);
      *(v4u*)(wp + (jn >> 2) * hi + (jn & 3) * 16) = o;
    }
    WAIT_L0();
    CFENCE();
#pragma unroll
    for (int f = 0; f < 8; ++f) R[f] = *(const v4u*)(lds + f * 1024 + l16);
  } else if (jb < 240) {
#pragma unroll
    for (int i = 0; i < 8; ++i) bst<SC1>(rs, l16, OFF_WD + ((w * 64 + jb - 176) * 8 + i) * 1024, R[i]);
  }
  SCHED();
  const unsigned gvo = (lane >> 5) * FSTR + (lane & 31) * 4;
  {
    const int xoff = q * 2048 + (wc * 64 + m) * 16;
    int wo[2];
#pragma unroll
    for (int kk = 0; kk < 2; ++kk) wo[kk] = 16384 + wr * 8192 + m * 128 + (((4 * kk + q) ^ ((m >> 1) & 7)) * 16);
    f32x4 acc[4][4], rsum = {0, 0, 0, 0};
#pragma unroll
    for (int i = 0; i < 4; ++i)
#pragma unroll
      for (int j = 0; j < 4; ++j) acc[i][j] = f32x4{0, 0, 0, 0};
    const v4u ones = {0x3f803f80u, 0x3f803f80u, 0x3f803f80u, 0x3f803f80u};
    v4u XA[4], WA[4], XB[4], WB[4];
    int sA = 0;
    auto rd = [&](int kk, v4u (&X)[4], v4u (&W)[4]) {
      W[0] = *(const v4u*)(shm + sA + wo[kk]);
      W[1] = *(const v4u*)(shm + sA + wo[kk] + 2048);
      W[2] = *(const v4u*)(shm + sA + wo[kk] + 6144 - wc * 2048);
      W[3] = *(const v4u*)(shm + sA + wo[kk] + 4096 + wc * 2048);
#pragma unroll
      for (int j = 0; j < 4; ++j) X[j] = *(const v4u*)(shm + sA + xoff + kk * 8192 + j * 256);
      if (kk) sA = (sA + STAGE) & (NS * STAGE - 1);
    };
    auto mm = [&](v4u (&X)[4], v4u (&W)[4]) {
#pragma unroll
      for (int j = 0; j < 4; ++j)
#pragma unroll
        for (int i = 0; i < 4; ++i) acc[i][j] = mfma(X[i], W[j], acc[i][j]);
      rsum = mfma(ones, W[3], rsum);
    };
    auto mix = [&]() {
#pragma unroll
      for (int i = 0; i < 8; ++i) {
        __builtin_amdgcn_sched_group_barrier(0x008, 1, 0);
        __builtin_amdgcn_sched_group_barrier(0x100, 1, 0);
      }
      __builtin_amdgcn_sched_group_barrier(0x008, 9, 0);
    };
    auto steps = [&](int n) {
#pragma unroll 1
      for (int t = 0; t < n; ++t) {
        SCHED();
        rd(1, XB, WB); mm(XA, WA); mix();
        SCHED();
        WAIT_L0();
        BAR();
        SCHED();
        rd(0, XA, WA); mm(XB, WB); mix();
      }
      SCHED();
    };
    BAR();
    rd(0, XA, WA);
    steps(2);
    WAIT_V0();
    if (lds_inc(C_JOB) == 3u) { if (lane < 8) flag_set(ws, FLINE(LN_G2 + lane * 8 + (b >> 5)) + (b & 31) * 4, tag); }
    steps(7);
    if (idx < 2) code_warm(idx * 4 + w, lane, L_DUMMY);
    const unsigned g2 = ld1(rs, gvo, FLINE(LN_G2 + xcd * 8 + w * 2));
    steps(2);
    if (__builtin_amdgcn_ballot_w64(g2 != tag) != 0ull) wait_words<2>(rs, gvo, FLINE(LN_G2 + xcd * 8 + w * 2), tag);
    CFENCE();
    steps(4);
    rd(1, XB, WB); mm(XA, WA); mix();
    SCHED();
    mm(XB, WB);
    f32x4 cav[4];
#pragma unroll
    for (int i = 0; i < 4; ++i) {
      const float* ml = (const float*)(shm + L_RAW + 6144) + wc * 64 + 32 * (i >> 1) + 8 * q + 4 * (i & 1);
      const f32x4 t = *(const f32x4*)ml + *(const f32x4*)(ml + 1792);
      const unsigned u0 = pk(t[0], t[1]), u1 = pk(t[2], t[3]);
      const unsigned z0 = pk(bflo(u0) + 1.f, bfhi(u0) + 1.f), z1 = pk(bflo(u1) + 1.f, bfhi(u1) + 1.f);
      cav[i] = f32x4{bflo(z0), bfhi(z0), bflo(z1), bfhi(z1)};
    }
#pragma unroll
    for (int jh = 0; jh < 2; ++jh) {
      const int gq = (2 * wr + jh + dq + 1) & 3;
      const float sg = (gq >> 1) ? -1.f : 1.f;
#pragma unroll
      for (int ip = 0; ip < 2; ++ip) {
        const f32x4 c0 = cav[2 * ip] * sg, c1 = cav[2 * ip + 1] * sg;
#pragma unroll
        for (int jl = 0; jl < 2; ++jl) {
          const int j = 2 * jh + jl;
          const int u = j < 2 ? j : (j == 3 ? wc : 1 - wc);
          const f32x4 a0 = acc[2 * ip][j] * c0, a1 = acc[2 * ip + 1][j] * c1;
          v4u o; o.x = pk(a0[0], a0[1]); o.y = pk(a0[2], a0[3]); o.z = pk(a1[0], a1[1]); o.w = pk(a1[2], a1[3]);
          const int tt = 4 * p + 2 * (gq & 1) + u, kb = (gq >> 1) * 16 + 4 * dq + 2 * wc + ip;
          bst<SC1>(rs, l16, OFF_PQ + ((tt * 32 + kb) * 2 + kh) * 1024, o);
        }
      }
    }
    if (wr == 1 && q < 2) st1(rs, lane * 4, OFF_RS + (p * 2 + (dq >> 1)) * 1024 + ((2 * (dq & 1) + wc) * 2 + kh) * 128, asu((dq >> 1) ? -rsum[0] : rsum[0]));
  }
  lds_add(C_RING);
  WAIT_V0();
  if (lane == 0) flag_set(ws, FLINE(LN_F1 + p) + (c * 4 + w) * 4, tag);
  if (idx < 2) code_warm(idx * 4 + w, lane, L_DUMMY);
  SCHED();
  if (w < 2) {
    if (jb < 176) {
      const int nkb = is1 ? NKB_X : NKB_H;
      const unsigned dst = (is1 ? OFF_W1 : OFF_W2) + ((jn0 >> 4) * nkb + (jk0 >> 5)) * 1024;
#pragma unroll
      for (int f = 0; f < 8; ++f) {
        const int ntl = f >> 1, kbl = f & 1;
        if (jk0 + kbl * 32 < (is1 ? D : F) && jn0 + (ntl >> 1) * 32 < (is1 ? F : D)) bst<SC1>(rs, l16, dst + (ntl * nkb + kbl) * 1024, R[f]);
      }
    }
    WAIT_V0();
    if (lds_inc(C_LATE) == 1u) { if (lane < 8) flag_set(ws, FLINE(LN_G3 + lane * 8 + (b >> 5)) + (b & 31) * 4, tag); }
  }
  wait_lds<true>(C_G3GO, 4);
  wait_words<16>(rs, gvo, FLINE(LN_G3 + xcd * 8 + w * 2), tag);
  lds_add(C_W1);
  const int ft0 = (c < 6 ? 11 * c : 66 + 10 * (c - 6)) + 3 * w;
  const int nft = w < 3 ? 3 : (c < 6 ? 2 : 1);
  constexpr int WK = 12;
  v4u w1f[WK][3];
  f32x4 accU[3];
  unsigned a1[3];
#pragma unroll
  for (int i = 0; i < 3; ++i) a1[i] = i < nft ? OFF_W1 + (ft0 + i) * NKB_X * 1024 : OOB;
  auto upd = [&](int k, v4u (&d)[3]) {
    const int kb = (k + 2 * c) & 15;
    const int ad = m == 8 ? L_MB + (512 + kb * 32 + q * 8) * 2 : (m == (kb >> 1) ? L_MB + (1024 + kb * 32 + q * 8) * 2 : L_ZERO);
    const v4u cf = *(const v4u*)(shm + ad);
#pragma unroll
    for (int i = 0; i < 3; ++i) accU[i] = mfma(cf, d[i], k == 0 ? f32x4{0, 0, 0, 0} : accU[i]);
  };
  wait_lds<true>(C_W1, 8);
#pragma unroll
  for (int k = 0; k < WK; ++k)
#pragma unroll
    for (int i = 0; i < 3; ++i) w1f[k][i] = bld<SC1>(rs, l16, a1[i] + ((k + 2 * c) & 15) * 1024);
  SCHED();
#pragma unroll
  for (int k = 0; k < WK; ++k) upd(k, w1f[k]);
  SCHED();
  wait_lds<true>(C_F2, 4);
#pragma unroll
  for (int i = 0; i < 3; ++i) {
    if (i < nft) {
#pragma unroll
      for (int k = WK; k < 16; ++k) glds16<SC1>(ws + a1[i] + ((k + 2 * c) & 15) * 1024 + l16, (LAS char*)shm + L_W1X + w * 12288 + ((k - WK) * 3 + i) * 1024);
    }
  }
  WAIT_V0();
  const char* w1x = shm + L_W1X + w * 12288 + l16;
#pragma unroll
  for (int k = WK; k < 16; ++k) {
    v4u t[3];
#pragma unroll
    for (int i = 0; i < 3; ++i) t[i] = *(const v4u*)(w1x + ((k - WK) * 3 + i) * 1024);
    upd(k, t);
  }
  v4u uf[3];
#pragma unroll
  for (int i = 0; i < 3; ++i) {
    const unsigned h0 = pk(accU[i][0], accU[i][1]), h1 = pk(accU[i][2], accU[i][3]);
    uf[i] = v4u{h0, h1, pk(accU[i][0] - bflo(h0), accU[i][1] - bfhi(h0)), pk(accU[i][2] - bflo(h1), accU[i][3] - bfhi(h1))};
  }
  SCHED();
  f32x4 acc[3][4];
  v4u hb[2][4];
  auto rdh = [&](int k) {
#pragma unroll
    for (int j = 0; j < 4; ++j) hb[k & 1][j] = *(const v4u*)(shm + L_H2 + j * H2W + k * 1024 + l16);
  };
  BAR();
  SCHED();
  rdh(0);
  static_for<0, 16>([&](auto ic) __attribute__((always_inline)) {
    constexpr int k = decltype(ic)::v;
    if (k == 3) BAR();
    SCHED();
    rdh(k + 1);
    if (k >= 1 && k <= 16 - WK) {
#pragma unroll
      for (int i = 0; i < 3; ++i) w1f[(k + WK - 1) % WK][i] = *(const v4u*)(w1x + ((k - 1) * 3 + i) * 1024);
    }
    SCHED();
#pragma unroll
    for (int j = 0; j < 4; ++j)
#pragma unroll
      for (int i = 0; i < 3; ++i) acc[i][j] = mfma(w1f[k % WK][i], hb[k & 1][j], k == 0 ? f32x4{0, 0, 0, 0} : acc[i][j]);
    SCHED();
  });
  float rsd[4];
#pragma unroll
  for (int j = 0; j < 4; ++j) rsd[j] = ((const float*)(shm + L_RSTD))[j * 16 + m];
#pragma unroll
  for (int i = 0; i < 3; ++i)
#pragma unroll
    for (int j = 0; j < 4; ++j) acc[i][j] = mfma(uf[i], hb[0][j], acc[i][j]);
  const unsigned vo = (q & 1) * (NKB_H * 1024) + ((q >> 1) * 16 + m) * 16;
#pragma unroll
  for (int i = 0; i < 3; ++i) {
    if (i < nft) {
      const int ft = ft0 + i;
#pragma unroll
      for (int j = 0; j < 4; j += 2) {
        const f32x4 a = acc[i][j] * rsd[j], bb = acc[i][j + 1] * rsd[j + 1];
        unsigned a0 = pk(fmaxf(a[0], 0.f), fmaxf(a[1], 0.f)), a1v = pk(fmaxf(a[2], 0.f), fmaxf(a[3], 0.f));
        unsigned b0 = pk(fmaxf(bb[0], 0.f), fmaxf(bb[1], 0.f)), b1 = pk(fmaxf(bb[2], 0.f), fmaxf(bb[3], 0.f));
        asm volatile("s_nop 1\n\tv_permlane16_swap_b32 %0, %2\n\tv_permlane16_swap_b32 %1, %3" : "+v"(a0), "+v"(a1v), "+v"(b0), "+v"(b1));
        bst<SC1>(rs, vo, OFF_H1 + ((4 * p + j) * NKB_H + (ft >> 1)) * 1024 + (ft & 1) * 512, v4u{a0, a1v, b0, b1});
      }
    }
  }
  WAIT_V0();
  if (lane == 0) flag_set(ws, FLINE(LN_F3 + p * 4 + (c >> 1)) + ((c & 1) * 4 + w) * 4, tag);
  lds_add(C_P4);
}

extern "C" void kernel_launch(void* const* d_in, const int* in_sizes, int n_in, void* d_out, int out_size, void* d_ws, size_t ws_size,
                              hipStream_t stream) {
  (void)hipFuncSetAttribute((const void*)fused_all, hipFuncAttributeMaxDynamicSharedMemorySize, K_LDS);
  fused_all<<<256, 512, K_LDS, stream>>>((const char*)d_in[0], (const char*)d_in[1], (const char*)d_in[2], (const char*)d_in[3], (const char*)d_in[4],
                                         (const char*)d_in[6], (const char*)d_in[7], (const char*)d_in[8], (const char*)d_in[9], (char*)d_ws, (char*)d_out);
}
```

```cpp
#include <hip/hip_runtime.h>
#include <hip/hip_bf16.h>
#include <stdint.h>

typedef unsigned v4u __attribute__((ext_vector_type(4)));
typedef unsigned v2u __attribute__((ext_vector_type(2)));
typedef float f32x4 __attribute__((ext_vector_type(4)));
typedef float f32x2 __attribute__((ext_vector_type(2)));
typedef short bf16x8 __attribute__((ext_vector_type(8)));
typedef __bf16 bf2 __attribute__((ext_vector_type(2)));
#define GAS __attribute__((address_space(1)))
#define LAS __attribute__((address_space(3)))
#define WAIT_V(n) asm volatile("s_waitcnt vmcnt(%0)" ::"n"(n) : "memory")
#define CFENCE() asm volatile("" ::: "memory")
#define WAIT_L0() do { CFENCE(); __builtin_amdgcn_s_waitcnt(0xC07F); } while (0)
#define WAIT_V0() do { WAIT_V(0); __builtin_amdgcn_s_waitcnt(0x0F70); CFENCE(); } while (0)
#define SCHED() __builtin_amdgcn_sched_barrier(0)
#define BAR() do { CFENCE(); __builtin_amdgcn_s_barrier(); CFENCE(); } while (0)
#define RSRC(p, n) __builtin_amdgcn_make_buffer_rsrc((void*)(p), 0, (n), 0x00020000)
constexpr int SC1 = 16, NT = 2;

constexpr int D = 512, F = 1376;
constexpr int NKB_X = 16, NKB_H = 43;
constexpr unsigned OFF_EPOCH = 0;
constexpr unsigned OFF_MOD  = 49152;
constexpr unsigned OFF_RS   = 131072;
constexpr unsigned OFF_ST   = 196608;
constexpr unsigned OFF_XNT  = 524288;
constexpr unsigned OFF_WD   = OFF_XNT + (2u << 20);
constexpr unsigned OFF_W1   = OFF_WD + (1u << 20);
constexpr unsigned OFF_W2   = OFF_W1 + 86 * 16 * 1024;
constexpr unsigned OFF_PQ   = OFF_W2 + 32 * 43 * 1024;
constexpr unsigned OFF_X1   = OFF_PQ + (8u << 20);
constexpr unsigned OFF_H1   = OFF_X1 + (2u << 20);
constexpr unsigned OFF_FL   = 24u << 20, FSTR = 256;
constexpr unsigned LN_G1 = 0, LN_G2 = 64, LN_G3 = 128;
constexpr unsigned LN_F1 = 192;
constexpr unsigned LN_F2 = 224, LN_F3 = 352;
constexpr unsigned LN_END = 480;
#define FLINE(l) (OFF_FL + (l) * FSTR)
constexpr unsigned WS_BYTES = FLINE(LN_END);
constexpr unsigned OOB = 0x4000000u;
constexpr int NS = 4, STAGE = 32768;
constexpr int L_TOP = NS * STAGE;
constexpr int L_RAW = L_TOP;
constexpr int L_MB = L_RAW + 14336;
constexpr int L_SL = L_MB + 3072;
constexpr int L_ZERO = L_TOP + 24576;
constexpr int L_CNT = L_ZERO + 64;
constexpr int L_RSTD = L_CNT + 64;
constexpr int L_DUMMY = L_RSTD + 256;
constexpr int K_LDS = L_DUMMY + 256;
constexpr int L_LN = 3 * STAGE;
constexpr int L_H2 = 0, H2W = 17 * 1024;
constexpr int L_W1X = 4 * H2W;
enum { C_G3GO = 0, C_G1 = 1, C_P4 = 2, C_JOB = 3, C_F1 = 4, C_MOD = 5, C_RING = 6, C_W1 = 7, C_F2 = 8, C_RED3 = 10, C_RED5 = 11, C_VEC = 13, C_LATE = 14 };
constexpr unsigned SPIN_CAP = 1u << 18;
constexpr int CODE_KB = 35;

template <int I> struct IC { static constexpr int v = I; };
template <int B, int E, class Fn> __device__ __forceinline__ void static_for(Fn&& f) { if constexpr (B < E) { f(IC<B>{}); static_for<B + 1, E>(f); } }
__device__ __forceinline__ float asf(unsigned u) { return __builtin_bit_cast(float, u); }
__device__ __forceinline__ unsigned asu(float f) { return __builtin_bit_cast(unsigned, f); }
__device__ __forceinline__ float bflo(unsigned u) { return asf(u << 16); }
__device__ __forceinline__ float bfhi(unsigned u) { return asf(u & 0xffff0000u); }
__device__ __forceinline__ unsigned pk(float lo, float hi) {
  f32x2 v = {lo, hi};
  return __builtin_bit_cast(unsigned, __builtin_convertvector(v, bf2));
}
__device__ __forceinline__ float rbf(float x) { return bflo(pk(x, x)); }
__device__ __forceinline__ f32x4 mfma(v4u a, v4u b, f32x4 c) {
  return __builtin_amdgcn_mfma_f32_16x16x32_bf16(__builtin_bit_cast(bf16x8, a), __builtin_bit_cast(bf16x8, b), c, 0, 0, 0);
}
template <int CTRL> __device__ __forceinline__ float dpp_add(float x) {
  int y = __builtin_amdgcn_update_dpp(0, __builtin_bit_cast(int, x), CTRL, 0xf, 0xf, false);
  return x + __builtin_bit_cast(float, y);
}
__device__ __forceinline__ float wave_sum(float x) {
  x = dpp_add<0x111>(x); x = dpp_add<0x112>(x); x = dpp_add<0x114>(x); x = dpp_add<0x118>(x);
  x = dpp_add<0x142>(x); x = dpp_add<0x143>(x);
  return __builtin_bit_cast(float, __builtin_amdgcn_readlane(__builtin_bit_cast(int, x), 63));
}
__device__ __forceinline__ float half_sum(float x) {
  float y = x;
  asm volatile("s_nop 1\n\tv_permlane32_swap_b32 %0, %1" : "+v"(x), "+v"(y));
  return x + y;
}
__device__ __forceinline__ float quad16_sum(float x) {
  float y = x;
  asm volatile("s_nop 1\n\tv_permlane16_swap_b32 %0, %1" : "+v"(x), "+v"(y));
  return half_sum(x + y);
}
__device__ __forceinline__ int fresh_lane() { int l; asm volatile("v_mbcnt_lo_u32_b32 %0, -1, 0\n\tv_mbcnt_hi_u32_b32 %0, -1, %0" : "=v"(l)); return l; }
template <int AUX> __device__ __forceinline__ v4u bld(__amdgpu_buffer_rsrc_t rs, unsigned voff, unsigned soff) { return __builtin_amdgcn_raw_buffer_load_b128(rs, voff, soff, AUX); }
template <int AUX> __device__ __forceinline__ void bst(__amdgpu_buffer_rsrc_t rs, unsigned voff, unsigned soff, v4u v) { __builtin_amdgcn_raw_buffer_store_b128(v, rs, voff + soff, 0, AUX); }
__device__ __forceinline__ void st1(__amdgpu_buffer_rsrc_t rs, unsigned voff, unsigned soff, unsigned v) { __builtin_amdgcn_raw_buffer_store_b32(v, rs, voff, soff, SC1); }
__device__ __forceinline__ unsigned ld1(__amdgpu_buffer_rsrc_t rs, unsigned voff, unsigned soff) { return __builtin_amdgcn_raw_buffer_load_b32(rs, voff, soff, SC1); }
template <int AUX> __device__ __forceinline__ void glds16(const char* g, LAS char* l) {
  __builtin_amdgcn_global_load_lds((const GAS unsigned*)g, (LAS unsigned*)l, 16, 0, AUX);
}
__device__ __forceinline__ void flag_set(char* ws, unsigned off, unsigned v) {
  asm volatile("global_atomic_swap %0, %1, %2 sc1" :: "v"(off), "v"(v), "s"(ws) : "memory");
}
template <int SLP> __device__ __forceinline__ void wait_words(__amdgpu_buffer_rsrc_t rs, unsigned voff, unsigned soff, unsigned tag) {
#pragma unroll 1
  for (unsigned spins = 0; spins < SPIN_CAP; ++spins) {
    CFENCE();
    const unsigned v = ld1(rs, voff, soff);
    if (__builtin_amdgcn_ballot_w64(v != tag) == 0ull) break;
    __builtin_amdgcn_s_sleep(SLP);
  }
  __builtin_amdgcn_fence(__ATOMIC_ACQUIRE, "wavefront");
  CFENCE();
  SCHED();
}
template <bool SLEEP> __device__ __forceinline__ void wait_lds(int idx, unsigned n) {
#pragma unroll 1
  for (unsigned spins = 0; spins < SPIN_CAP * 64; ++spins) {
    unsigned v;
    asm volatile("ds_read_b32 %0, %1\n\ts_waitcnt lgkmcnt(0)" : "=v"(v) : "v"(L_CNT + idx * 4) : "memory");
    if (__builtin_amdgcn_readfirstlane(v) >= n) break;
    if (SLEEP) __builtin_amdgcn_s_sleep(1);
  }
  SCHED();
}
__device__ __forceinline__ void lds_add(int idx) {
  unsigned long long ex;
  asm volatile("s_mov_b64 %0, exec\n\ts_mov_b64 exec, 1\n\tds_add_u32 %1, %2\n\ts_mov_b64 exec, %0" : "=&s"(ex) : "v"(L_CNT + idx * 4), "v"(1u) : "memory");
}
__device__ __forceinline__ unsigned lds_inc(int idx) {
  unsigned long long ex; unsigned old;
  asm volatile("s_mov_b64 %0, exec\n\ts_mov_b64 exec, 1\n\tds_add_rtn_u32 %1, %2, %3\n\ts_waitcnt lgkmcnt(0)\n\ts_mov_b64 exec, %0"
               : "=&s"(ex), "=&v"(old) : "v"(L_CNT + idx * 4), "v"(1u) : "memory");
  return __builtin_amdgcn_readfirstlane(old);
}

__global__ __launch_bounds__(512, 2) void fused_all(const char* __restrict__ x, const char* __restrict__ wsr, const char* __restrict__ wsi,
                                                    const char* __restrict__ wdr, const char* __restrict__ wdi,
                                                    const char* __restrict__ w1, const char* __restrict__ w2, const char* __restrict__ cvec,
                                                    const char* __restrict__ wmod, char* __restrict__ ws, char* __restrict__ out) {
  extern __shared__ __attribute__((aligned(16))) char shm[];
  const unsigned long long t00 = __builtin_amdgcn_s_memrealtime();
  const int b = blockIdx.x, xcd = b & 7, idx = b >> 3, pl = idx >> 3, p = xcd * 4 + pl, c = idx & 7, dq = c & 3, kh = c >> 2;
  const int w8 = __builtin_amdgcn_readfirstlane(threadIdx.x >> 6), w = w8 & 3;
  const __amdgpu_buffer_rsrc_t rs = RSRC(ws, WS_BYTES);
  const float* sll = (const float*)(shm + L_SL);

  unsigned long long pc0;
  asm volatile("s_getpc_b64 %0" : "=s"(pc0));
  auto code_warm = [&](int piece, int lane, int ldsoff) {
    const unsigned sl = piece * 64 + lane;
    if (sl < CODE_KB * 8) __builtin_amdgcn_global_load_lds((const GAS unsigned*)((pc0 & ~127ull) + (unsigned long long)sl * 128), (LAS unsigned*)((LAS char*)shm + ldsoff), 4, 0, 0);
  };
  if (idx == 0 && w8 < 5) code_warm(w8, threadIdx.x & 63, L_LN + 8192 + w8 * 256);
  SCHED();
  v4u xq; unsigned tq;
  asm volatile("global_load_dwordx4 %0, %2, %3\n\tglobal_load_dword %1, %4, %5 sc1"
               : "=&v"(xq), "=&v"(tq) : "v"(b * 8192 + threadIdx.x * 16), "s"(x), "v"(OFF_EPOCH), "s"(ws) : "memory");
  SCHED();
  v4u R[8], cvv;
  const int jb = c * 32 + pl * 8 + xcd;
  const int jm = ((c * 2 + (pl >> 1)) * 8 + xcd) * 2 + (pl & 1);
  const bool is1 = w == 0;
  const int jk0 = (is1 ? (jb & 7) : (jb >> 3)) * 64, jn0 = (is1 ? (jb >> 3) : (jb & 7)) * 64;
  {
    const int lane = threadIdx.x & 63, m = lane & 15, q = lane >> 4, kg = lane >> 3, ng = lane & 7;
    const char* base; unsigned off, ld, len;
    if (w8 >= 4 || (w >= 2 ? jm >= 192 : jb >= 240)) { base = cvec; ld = 0; off = 0; len = 0; }
    else if (w >= 2) { base = wmod; ld = 6144; len = 512 * 6144; off = ((jm / 96) * 256 + (w & 1) * 128 + (lane >> 2) * 8) * 6144 + (jm % 96) * 64 + (lane & 3) * 16; }
    else if (jb < 176) {
      base = is1 ? w1 : w2; ld = is1 ? F * 2 : D * 2; len = D * F * 2;
      const bool ok = (jk0 + kg * 8 < (is1 ? D : F)) && (jn0 + ng * 8 < (is1 ? F : D));
      off = ok ? (jk0 + kg * 8) * ld + (jn0 + ng * 8) * 2 : OOB;
    } else {
      const int j3 = w * 64 + jb - 176, et = j3 >> 2, kb0 = (j3 & 3) * 8;
      base = kb0 < 16 ? wdr : wdi; ld = 64; len = D * D * 2;
      off = (32 * (et >> 1) + 8 * (m >> 2) + 4 * (et & 1) + (m & 3)) * 1024 + (kb0 & 15) * 64 + q * 16;
    }
    ld = __builtin_amdgcn_readfirstlane(ld);
    len = __builtin_amdgcn_readfirstlane(len);
    const __amdgpu_buffer_rsrc_t rj = RSRC(base, len);
#pragma unroll
    for (int k = 0; k < 8; ++k) R[k] = bld<NT>(rj, off, k * ld);
    cvv = bld<0>(RSRC(cvec, 1024), ((w & 1) * 128 + (lane >> 2) * 8) * 2, (jm / 96 & 1) * 512);
    SCHED();
  }
  asm volatile("s_waitcnt vmcnt(9)" : "+v"(xq), "+v"(tq) :: "memory");
  unsigned tag;
  {
    const int tid = threadIdx.x, lane = tid & 63;
    float v[8];
#pragma unroll
    for (int u = 0; u < 4; ++u) { v[2 * u] = bflo(xq[u]); v[2 * u + 1] = bfhi(xq[u]); }
    const float mean = wave_sum(((v[0] + v[1]) + (v[2] + v[3])) + ((v[4] + v[5]) + (v[6] + v[7]))) * (1.f / D);
    float ss = 0.f;
#pragma unroll
    for (int j = 0; j < 8; ++j) { const float t = v[j] - mean; ss += t * t; }
    const float var = wave_sum(ss) * (1.f / D);
    const float mb = rbf(mean);
    const float rsd = 1.f / rbf(__builtin_sqrtf(rbf(rbf(var) + 1e-5f)));
    v4u o;
#pragma unroll
    for (int u = 0; u < 4; ++u) {
      const unsigned d2 = pk(v[2 * u] - mb, v[2 * u + 1] - mb);
      o[u] = pk(bflo(d2) * rsd, bfhi(d2) * rsd);
    }
    *(v4u*)(shm + L_LN + tid * 16) = o;
    if (tid < 32) ((unsigned*)(shm + L_ZERO))[tid] = 0u;
    WAIT_L0();
    BAR();
    const int d = (tid & ~31) + 8 * ((tid >> 2) & 3) + 4 * ((tid >> 4) & 1) + (tid & 3);
    const unsigned short* sp = (const unsigned short*)(shm + L_LN) + d;
    v4u t;
#pragma unroll
    for (int u = 0; u < 4; ++u) t[u] = (unsigned)sp[(2 * u) * 512] | ((unsigned)sp[(2 * u + 1) * 512] << 16);
    tag = __builtin_amdgcn_readfirstlane(tq) * 0x9E3779B1u + 0x7F4A7C15u;
    bst<SC1>(rs, tid * 16, OFF_XNT + b * 8192, t);
    WAIT_V0();
    BAR();
    if (w8 == 0) { if (lane < 8) flag_set(ws, FLINE(LN_G1 + lane * 8 + (b >> 5)) + (b & 31) * 4, tag); }
    else __builtin_amdgcn_s_sleep(1);
  }

  if (w8 >= 4) {
    const int lane = fresh_lane(), m = lane & 15, q = lane >> 4;
    const unsigned l16 = lane * 16;
    v4u wd[8][4], res[2];
    {
      const int gq = (w + dq + 1) & 3;
      const char* wbase = (gq < 2 ? wsr : wsi) + (size_t)(p * 64 + (gq & 1) * 32 + (lane >> 3)) * 4096 + kh * 2048;
      const int sl = lane & 7, h = lane >> 4;
      const char* wsrc0 = wbase + ((sl ^ h) * 16);
      const char* wsrc1 = wbase + ((sl ^ (4 + h)) * 16) + 8 * 4096;
      const char* xsrc = ws + OFF_XNT + ((size_t)(kh * 128 + 2 * w) * 512 + dq * 128 + lane) * 16;
      auto issW = [&](int st) {
        LAS char* dst = (LAS char*)shm + (st & 3) * STAGE + w * 4096 + 16384;
#pragma unroll
        for (int i = 0; i < 4; ++i) glds16<NT>(((i & 1) ? wsrc1 : wsrc0) + (size_t)(i >> 1) * 16 * 4096 + st * 128, dst + i * 1024);
      };
      auto issX = [&](int st) {
        LAS char* dst = (LAS char*)shm + (st & 3) * STAGE + w * 4096;
#pragma unroll
        for (int i = 0; i < 4; ++i) glds16<SC1>(xsrc + (size_t)st * 65536 + (i >> 1) * 8192 + (i & 1) * 1024, dst + i * 1024);
      };
      issW(0); issW(1); issW(2); issW(3);
#pragma unroll
      for (int ip = 0; ip < 2; ++ip) res[ip] = bld<0>(RSRC(x, 2048 * 1024), m * 1024 + q * 16 + ip * 64, (p * 64 + w * 16) * 1024 + c * 128);
      SCHED();
      if (w < 2) { wait_words<2>(rs, (lane >> 5) * FSTR + (lane & 31) * 4, FLINE(LN_G1 + xcd * 8 + kh * 4 + w * 2), tag); lds_add(C_G1); }
      wait_lds<true>(C_G1, 2);
      issX(0); issX(1); issX(2); issX(3);
      WAIT_V(12); BAR();
      WAIT_V(8); BAR();
      issX(4);
      WAIT_V(8); BAR();
      issW(4); issW(5);
      WAIT_V(12); BAR();
      issX(5); issX(6);
#pragma unroll 1
      for (int t = 3; t < 11; t += 2) {
        WAIT_V(12); BAR();
        issW(t + 3); issW(t + 4);
        WAIT_V(12); BAR();
        issX(t + 4); issX(t + 5);
      }
      WAIT_V(12); BAR();
      issW(14); issW(15);
      const unsigned a0 = OFF_WD + ((4 * c) * 32 + 8 * w) * 1024;
      auto ldwd = [&](int f0, int f1) {
        SCHED();
#pragma unroll
        for (int f = f0; f < f1; ++f) wd[f >> 2][f & 3] = bld<SC1>(rs, l16, a0 + ((f & 3) * 32 + (f >> 2)) * 1024);
        SCHED();
      };
      WAIT_V(12); BAR();
      issX(15);
      {
        const char* mp = ws + OFF_MOD + (w & 1) * 12288;
        LAS char* dst = (LAS char*)shm + L_RAW + (w & 1) * 7168;
        const unsigned sl_off = lane < 32 ? 2048 + dq * 512 + l16 : (lane < 48 ? 4096 + c * 256 + l16 - 512 : 10240 + c * 256 + l16 - 768);
#pragma unroll
        for (int i = 0; i < 4; ++i) {
          const int pc = (w >> 1) * 4 + i;
          const unsigned so = pc < 2 ? pc * 1024 + l16 : (pc < 6 ? 4096 + pc * 1024 + l16 : sl_off);
          glds16<SC1>(mp + so, dst + (pc < 6 ? pc : 6) * 1024);
        }
      }
      WAIT_V(12); BAR();
      ldwd(0, 12);
      WAIT_V(12); BAR();
      ldwd(12, 24);
      {
        const int tid = w * 64 + lane;
        f32x2 pa[3], pb[3];
        float sa1, sb1;
        asm volatile("ds_read_b64 %0, %8\n\tds_read_b64 %1, %8 offset:2048\n\tds_read_b64 %2, %8 offset:4096\n\t"
                     "ds_read_b64 %3, %8 offset:7168\n\tds_read_b64 %4, %8 offset:9216\n\tds_read_b64 %5, %8 offset:11264\n\t"
                     "ds_read_b32 %6, %9\n\tds_read_b32 %7, %9 offset:7168\n\ts_waitcnt lgkmcnt(0)"
                     : "=&v"(pa[0]), "=&v"(pa[1]), "=&v"(pa[2]), "=&v"(pb[0]), "=&v"(pb[1]), "=&v"(pb[2]), "=&v"(sa1), "=&v"(sb1)
                     : "v"(L_RAW + tid * 8), "v"(tid < 128 ? L_RAW + 6656 + tid * 4 : L_RAW + 4096 + c * 256 + (tid & 63) * 4) : "memory");
        unsigned z[3];
#pragma unroll
        for (int i = 0; i < 3; ++i) {
          z[i] = pk(pa[i][0] + pb[i][0], pa[i][1] + pb[i][1]);
          if (i == 2) z[i] = pk(bflo(z[i]) + 1.f, bfhi(z[i]) + 1.f);
        }
        float sv = rbf(sa1 + sb1);
        if (tid >= 128) sv = rbf(sv + 1.f);
        asm volatile("ds_write_b32 %0, %1\n\tds_write_b32 %0, %2 offset:1024\n\tds_write_b32 %0, %3 offset:2048\n\tds_write_b32 %0, %4 offset:3072"
                     :: "v"(L_MB + tid * 4), "v"(z[0]), "v"(z[1]), "v"(z[2]), "v"(sv) : "memory");
        lds_add(C_VEC);
      }
      wait_lds<true>(C_RING, 4);
      ldwd(24, 32);
      wait_lds<true>(C_VEC, 4);
    }
    SCHED();
    f32x4 acc[4], oth[4];
    f32x4* red = (f32x4*)(shm + L_H2);
    auto put = [&](int j, int ln) {
#pragma unroll
      for (int i = 0; i < 4; ++i) red[((j + w) & 3) * (H2W / 16) + ((j - 1) * 4 + i) * 64 + ln] = acc[i];
    };
    auto get = [&](int ln) {
#pragma unroll
      for (int i = 0; i < 4; ++i)
        oth[i] = (red[w * (H2W / 16) + i * 64 + ln] + red[w * (H2W / 16) + (4 + i) * 64 + ln]) + red[w * (H2W / 16) + (8 + i) * 64 + ln];
    };
    {
      f32x4 accS[4];
      v4u sf[8];
#pragma unroll
      for (int k = 0; k < 8; ++k) sf[k] = *(const v4u*)(shm + L_MB + ((8 * (w & 1) + k) * 32 + q * 8) * 2);
      SCHED();
#pragma unroll
      for (int k = 0; k < 8; ++k)
#pragma unroll
        for (int i = 0; i < 4; ++i) accS[i] = mfma(wd[k][i], sf[k], k == 0 ? f32x4{0, 0, 0, 0} : accS[i]);
      SCHED();
      if (w == 0) { wait_words<2>(rs, (lane & 31) * 4, FLINE(LN_F1 + p), tag); lds_add(C_F1); }
      else wait_lds<true>(C_F1, 1);
      constexpr int WIN = 12;
      f32x2 rr[4];
      v4u pq[WIN];
      unsigned b0[4];
#pragma unroll
      for (int j = 0; j < 4; ++j) b0[j] = OFF_PQ + ((4 * p + ((j + w) & 3)) * 64 + 16 * w) * 1024;
      auto ldr = [&](int j) {
#pragma unroll
        for (int h = 0; h < 2; ++h) rr[j][h] = asf(ld1(rs, m * 4 + h * 128, OFF_RS + (p * 2 + (w >> 1)) * 1024 + ((j + w) & 3) * 256));
      };
      auto ldp = [&](int n) { pq[n % WIN] = bld<SC1>(rs, l16, b0[3 - (n >> 4)] + (n & 15) * 1024); };
      ldr(3);
      SCHED();
#pragma unroll
      for (int n = 0; n < WIN; ++n) ldp(n);
      SCHED();
      ldr(2); ldr(1); ldr(0);
      SCHED();
      static_for<0, 64>([&](auto ic) __attribute__((always_inline)) {
        constexpr int n = decltype(ic)::v, j = 3 - (n >> 4), g = n & 15;
        const v4u cur = pq[n % WIN];
        if (g == 0 && j < 3) {
          f32x4 nx[4];
#pragma unroll
          for (int i = 0; i < 4; ++i) nx[i] = mfma(wd[0][i], cur, accS[i] * (rr[j][0] + rr[j][1]));
          put(j + 1, lane);
          if (j == 0) lds_add(C_RED3);
#pragma unroll
          for (int i = 0; i < 4; ++i) acc[i] = nx[i];
        } else {
#pragma unroll
          for (int i = 0; i < 4; ++i) acc[i] = mfma(wd[g >> 1][i], cur, n == 0 ? accS[i] * (rr[j][0] + rr[j][1]) : acc[i]);
        }
        SCHED();
        if (n + WIN < 64) ldp(n + WIN);
        if (n + WIN == 63) lds_add(C_W1);
        if (n == 40) lds_add(C_G3GO);
        if (n == 58) { asm volatile("" : "+v"(acc[0]), "+v"(acc[1]), "+v"(acc[2]), "+v"(acc[3])); wait_lds<false>(C_RED3, 4); get(lane); }
        SCHED();
      });
    }
    auto gate = [&](const f32x4 (&gv)[4]) {
#pragma unroll
      for (int i = 0; i < 4; ++i) {
        const f32x4 t = (acc[i] + oth[i]) * gv[i];
        const unsigned r0 = res[i >> 1][2 * (i & 1)], r1 = res[i >> 1][2 * (i & 1) + 1];
        res[i >> 1][2 * (i & 1)] = pk(bflo(r0) + t[0], bfhi(r0) + t[1]);
        res[i >> 1][2 * (i & 1) + 1] = pk(bflo(r1) + t[2], bfhi(r1) + t[3]);
      }
    };
    const unsigned xb = OFF_X1 + (4 * p + w) * NKB_X * 1024, sb0 = OFF_ST + (p * 4 + w) * 1024;
    {
      f32x4 cv[4], gv[4];
#pragma unroll
      for (int i = 0; i < 4; ++i) { gv[i] = *(const f32x4*)(sll + 32 * (i >> 1) + 8 * q + 4 * (i & 1)); cv[i] = *(const f32x4*)(sll + 128 + 32 * (i >> 1) + 8 * q + 4 * (i & 1)); }
      gate(gv);
      float v[16];
#pragma unroll
      for (int u = 0; u < 8; ++u) { v[2 * u] = bflo(res[u >> 2][u & 3]); v[2 * u + 1] = bfhi(res[u >> 2][u & 3]); }
      float s = 0.f;
#pragma unroll
      for (int u = 0; u < 16; u += 4) s += (v[u] + v[u + 1]) + (v[u + 2] + v[u + 3]);
      const float mc = quad16_sum(s) * (1.f / 64);
#pragma unroll
      for (int u = 0; u < 16; ++u) v[u] -= mc;
#pragma unroll
      for (int ip = 0; ip < 2; ++ip) {
        v4u o;
#pragma unroll
        for (int u = 0; u < 4; ++u) o[u] = pk(v[8 * ip + 2 * u] * cv[2 * ip + (u >> 1)][(2 * u) & 3], v[8 * ip + 2 * u + 1] * cv[2 * ip + (u >> 1)][(2 * u + 1) & 3]);
        bst<SC1>(rs, l16, xb + (2 * c + ip) * 1024, o);
        *(v4u*)(shm + L_H2 + w * H2W + ip * 1024 + l16) = o;
      }
      float ss = 0.f, s2 = 0.f;
#pragma unroll
      for (int u = 0; u < 16; u += 2) { ss += v[u] * v[u]; s2 += v[u + 1] * v[u + 1]; }
      ss = quad16_sum(ss + s2);
      if (q == 0) __builtin_amdgcn_raw_buffer_store_b64(v2u{asu(mc), asu(ss)}, rs, m * 8, sb0 + c * 128, SC1);
      WAIT_V0();
      if (lane == 0) flag_set(ws, FLINE(LN_F2 + p * 4 + w) + c * 4, tag);
      lds_add(C_F2);
    }
    asm volatile("" : "+v"(res[0]), "+v"(res[1]));
    SCHED();
    __builtin_amdgcn_s_sleep(10);
    wait_words<2>(rs, (lane & 7) * 4, FLINE(LN_F2 + p * 4 + w), tag);
    {
      v4u X[14];
      v2u st[8];
#pragma unroll
      for (int k = 0; k < 14; ++k) X[k] = bld<SC1>(rs, l16, xb + ((2 * c + 2 + k) & 15) * 1024);
#pragma unroll
      for (int i = 0; i < 8; ++i) st[i] = __builtin_amdgcn_raw_buffer_load_b64(rs, m * 8 + i * 128, sb0, SC1);
      SCHED();
#pragma unroll
      for (int k = 0; k < 14; ++k) {
        *(v4u*)(shm + L_H2 + w * H2W + (2 + k) * 1024 + l16) = X[k];
        if (k == 1) { WAIT_L0(); BAR(); SCHED(); }
      }
      float mu[8], mean = 0.f, m2 = 0.f;
#pragma unroll
      for (int i = 0; i < 8; ++i) { mu[i] = asf(st[i].x); mean += mu[i]; m2 += asf(st[i].y); }
      mean *= 0.125f;
      float dv = 0.f;
#pragma unroll
      for (int i = 0; i < 8; ++i) { const float d0 = mu[i] - mean; dv += d0 * d0; }
      m2 += 64.f * dv;
      const float den = rbf(__builtin_sqrtf(rbf(rbf(m2 * (1.f / D)) + 1e-5f)));
      const float mb = rbf(mean);
      unsigned d0 = q == 0 ? pk(mu[0] - mb, mu[1] - mb) : pk(mu[4] - mb, mu[5] - mb);
      unsigned d1 = q == 0 ? pk(mu[2] - mb, mu[3] - mb) : pk(mu[6] - mb, mu[7] - mb);
      if (q >= 2) { d0 = q == 2 ? pk(den, 0.f) : 0u; d1 = 0u; }
      *(v4u*)(shm + L_H2 + w * H2W + 16 * 1024 + l16) = v4u{d0, d1, d0, d1};
      if (q == 0) ((float*)(shm + L_RSTD))[w * 16 + m] = 1.f / den;
      WAIT_L0(); BAR(); SCHED();
    }
    {
      constexpr int WIN = 8;
      v4u w2f[11][4];
      const unsigned a0 = OFF_W2 + ((4 * c) * NKB_H + 11 * w) * 1024;
      const unsigned last = w == 3 ? OOB : 10 * 1024;
#pragma unroll
      for (int k = 0; k < 11; ++k)
#pragma unroll
        for (int i = 0; i < 4; ++i) w2f[k][i] = bld<SC1>(rs, l16, a0 + i * NKB_H * 1024 + (k < 10 ? k * 1024 : last));
      SCHED();
      wait_lds<true>(C_P4, 4);
      const int ln = fresh_lane();
      __builtin_amdgcn_s_sleep(14);
      wait_words<2>(rs, (ln & 7) * 4, FLINE(LN_F3 + p * 4 + w), tag);
      v4u hf[WIN];
      unsigned b0[4];
#pragma unroll
      for (int j = 0; j < 4; ++j) b0[j] = OFF_H1 + ((4 * p + ((j + w) & 3)) * NKB_H + 11 * w) * 1024;
      auto ldh = [&](int n) { hf[n % WIN] = bld<SC1>(rs, ln * 16, b0[3 - n / 11] + (n % 11 < 10 ? (n % 11) * 1024 : last)); };
#pragma unroll
      for (int n = 0; n < WIN; ++n) ldh(n);
      SCHED();
      static_for<0, 44>([&](auto ic) __attribute__((always_inline)) {
        constexpr int n = decltype(ic)::v, j = 3 - n / 11, g = n % 11;
        const v4u cur = hf[n % WIN];
        if (g == 0 && j < 3) {
          f32x4 nx[4];
#pragma unroll
          for (int i = 0; i < 4; ++i) nx[i] = mfma(w2f[0][i], cur, f32x4{0, 0, 0, 0});
          put(j + 1, ln);
          if (j == 0) lds_add(C_RED5);
#pragma unroll
          for (int i = 0; i < 4; ++i) acc[i] = nx[i];
        } else {
#pragma unroll
          for (int i = 0; i < 4; ++i) acc[i] = mfma(w2f[g][i], cur, n == 0 ? f32x4{0, 0, 0, 0} : acc[i]);
        }
        SCHED();
        if (n + WIN < 44) ldh(n + WIN);
        if (n == 40) { asm volatile("" : "+v"(acc[0]), "+v"(acc[1]), "+v"(acc[2]), "+v"(acc[3])); wait_lds<false>(C_RED5, 4); get(ln); }
        SCHED();
      });
      f32x4 gv[4];
#pragma unroll
      for (int i = 0; i < 4; ++i) gv[i] = *(const f32x4*)(sll + 64 + 32 * (i >> 1) + 8 * (ln >> 4) + 4 * (i & 1));
      gate(gv);
#pragma unroll
      for (int ip = 0; ip < 2; ++ip) bst<0>(RSRC(out, 2048 * 1024), (ln & 15) * 1024 + (ln >> 4) * 16 + ip * 64, (p * 64 + w * 16) * 1024 + c * 128, res[ip]);
      if (b == 0 && w == 0 && ln == 0) st1(rs, 0, OFF_EPOCH, tag);
    }
    while (__builtin_amdgcn_s_memrealtime() - t00 < 3000) __builtin_amdgcn_s_sleep(1);
    return;
  }

  const int lane = fresh_lane(), m = lane & 15, q = lane >> 4;
  const unsigned l16 = lane * 16;
  const int wr = w >> 1, wc = w & 1;
  if (w >= 2) {
    if (jm < 192) {
      float a8[8] = {0.f, 0.f, 0.f, 0.f, 0.f, 0.f, 0.f, 0.f};
#pragma unroll
      for (int i = 0; i < 8; ++i) {
        const float c0 = (i & 1) ? bfhi(cvv[i >> 1]) : bflo(cvv[i >> 1]);
        const float sl = rbf(c0 / (1.f + __expf(-c0)));
#pragma unroll
        for (int u = 0; u < 4; ++u) { a8[2 * u] += sl * bflo(R[i][u]); a8[2 * u + 1] += sl * bfhi(R[i][u]); }
      }
      float* red = (float*)(shm + L_TOP + 16384);
      *(f32x4*)(red + ((w & 1) * 16 + (lane >> 2)) * 36 + (lane & 3) * 8) = f32x4{a8[0], a8[1], a8[2], a8[3]};
      *(f32x4*)(red + ((w & 1) * 16 + (lane >> 2)) * 36 + (lane & 3) * 8 + 4) = f32x4{a8[4], a8[5], a8[6], a8[7]};
      if (lds_inc(C_MOD) == 1u) {
        float s = 0.f;
#pragma unroll
        for (int i = 0; i < 16; ++i) s += red[((lane >> 5) * 16 + i) * 36 + (lane & 31)];
        s = half_sum(s);
        if (lane < 32) st1(rs, lane * 4, OFF_MOD + ((jm / 96) * 3072 + (jm % 96) * 32) * 4, asu(s));
      }
    }
  } else if (jb < 176) {
    const int kg = lane >> 3, ng = lane & 7;
    char* lds = shm + L_TOP + w * 8192;
    const int a = ng & 3, qq = kg & 3;
    char* wp = lds + ((ng >> 2) * 4 + (kg >> 2)) * 1024 + (is1 ? (a >> 1) * 2048 + (qq * 16 + 8 * (a & 1)) * 16 : (qq * 16 + 4 * a) * 16);
    const int hi = is1 ? 64 : 2048;
#pragma unroll
    for (int jn = 0; jn < 8; ++jn) {
      const unsigned sel = (jn & 1) ? 0x07060302u : 0x05040100u;
      const int u = jn >> 1;
      v4u o;
      o.x = __builtin_amdgcn_perm(R[1][u], R[0][u], sel);
      o.y = __builtin_amdgcn_perm(R[3][u], R[2][u], sel);
      o.z = __builtin_amdgcn_perm(R[5][u], R[4][u], sel);
      o.w = __builtin_amdgcn_perm(R[7][u], R[6][u], sel);
      *(v4u*)(wp + (jn >> 2) * hi + (jn & 3) * 16) = o;
    }
    WAIT_L0();
    CFENCE();
#pragma unroll
    for (int f = 0; f < 8; ++f) R[f] = *(const v4u*)(lds + f * 1024 + l16);
  } else if (jb < 240) {
#pragma unroll
    for (int i = 0; i < 8; ++i) bst<SC1>(rs, l16, OFF_WD + ((w * 64 + jb - 176) * 8 + i) * 1024, R[i]);
  }
  SCHED();
  const unsigned gvo = (lane >> 5) * FSTR + (lane & 31) * 4;
  {
    const int xoff = q * 2048 + (wc * 64 + m) * 16;
    int wo[2];
#pragma unroll
    for (int kk = 0; kk < 2; ++kk) wo[kk] = 16384 + wr * 8192 + m * 128 + (((4 * kk + q) ^ ((m >> 1) & 7)) * 16);
    f32x4 acc[4][4], rsum = {0, 0, 0, 0};
#pragma unroll
    for (int i = 0; i < 4; ++i)
#pragma unroll
      for (int j = 0; j < 4; ++j) acc[i][j] = f32x4{0, 0, 0, 0};
    const v4u ones = {0x3f803f80u, 0x3f803f80u, 0x3f803f80u, 0x3f803f80u};
    v4u XA[4], WA[4], XB[4], WB[4];
    int sA = 0;
    auto rd = [&](int kk, v4u (&X)[4], v4u (&W)[4]) {
      W[0] = *(const v4u*)(shm + sA + wo[kk]);
      W[1] = *(const v4u*)(shm + sA + wo[kk] + 2048);
      W[2] = *(const v4u*)(shm + sA + wo[kk] + 6144 - wc * 2048);
      W[3] = *(const v4u*)(shm + sA + wo[kk] + 4096 + wc * 2048);
#pragma unroll
      for (int j = 0; j < 4; ++j) X[j] = *(const v4u*)(shm + sA + xoff + kk * 8192 + j * 256);
      if (kk) sA = (sA + STAGE) & (NS * STAGE - 1);
    };
    auto mm = [&](v4u (&X)[4], v4u (&W)[4]) {
#pragma unroll
      for (int j = 0; j < 4; ++j)
#pragma unroll
        for (int i = 0; i < 4; ++i) acc[i][j] = mfma(X[i], W[j], acc[i][j]);
      rsum = mfma(ones, W[3], rsum);
    };
    auto mix = [&]() {
#pragma unroll
      for (int i = 0; i < 8; ++i) {
        __builtin_amdgcn_sched_group_barrier(0x008, 1, 0);
        __builtin_amdgcn_sched_group_barrier(0x100, 1, 0);
      }
      __builtin_amdgcn_sched_group_barrier(0x008, 9, 0);
    };
    auto steps = [&](int n) {
#pragma unroll 1
      for (int t = 0; t < n; ++t) {
        SCHED();
        rd(1, XB, WB); mm(XA, WA); mix();
        SCHED();
        WAIT_L0();
        BAR();
        SCHED();
        rd(0, XA, WA); mm(XB, WB); mix();
      }
      SCHED();
    };
    BAR();
    rd(0, XA, WA);
    steps(2);
    WAIT_V0();
    if (lds_inc(C_JOB) == 3u) { if (lane < 8) flag_set(ws, FLINE(LN_G2 + lane * 8 + (b >> 5)) + (b & 31) * 4, tag); }
    steps(7);
    if (idx < 2) code_warm(idx * 4 + w, lane, L_DUMMY);
    const unsigned g2 = ld1(rs, gvo, FLINE(LN_G2 + xcd * 8 + w * 2));
    steps(2);
    if (__builtin_amdgcn_ballot_w64(g2 != tag) != 0ull) wait_words<2>(rs, gvo, FLINE(LN_G2 + xcd * 8 + w * 2), tag);
    CFENCE();
    steps(4);
    rd(1, XB, WB); mm(XA, WA); mix();
    SCHED();
    mm(XB, WB);
    f32x4 cav[4];
#pragma unroll
    for (int i = 0; i < 4; ++i) {
      const float* ml = (const float*)(shm + L_RAW + 6144) + wc * 64 + 32 * (i >> 1) + 8 * q + 4 * (i & 1);
      const f32x4 t = *(const f32x4*)ml + *(const f32x4*)(ml + 1792);
      const unsigned u0 = pk(t[0], t[1]), u1 = pk(t[2], t[3]);
      const unsigned z0 = pk(bflo(u0) + 1.f, bfhi(u0) + 1.f), z1 = pk(bflo(u1) + 1.f, bfhi(u1) + 1.f);
      cav[i] = f32x4{bflo(z0), bfhi(z0), bflo(z1), bfhi(z1)};
    }
#pragma unroll
    for (int jh = 0; jh < 2; ++jh) {
      const int gq = (2 * wr + jh + dq + 1) & 3;
      const float sg = (gq >> 1) ? -1.f : 1.f;
#pragma unroll
      for (int ip = 0; ip < 2; ++ip) {
        const f32x4 c0 = cav[2 * ip] * sg, c1 = cav[2 * ip + 1] * sg;
#pragma unroll
        for (int jl = 0; jl < 2; ++jl) {
          const int j = 2 * jh + jl;
          const int u = j < 2 ? j : (j == 3 ? wc : 1 - wc);
          const f32x4 a0 = acc[2 * ip][j] * c0, a1 = acc[2 * ip + 1][j] * c1;
          v4u o; o.x = pk(a0[0], a0[1]); o.y = pk(a0[2], a0[3]); o.z = pk(a1[0], a1[1]); o.w = pk(a1[2], a1[3]);
          const int tt = 4 * p + 2 * (gq & 1) + u, kb = (gq >> 1) * 16 + 4 * dq + 2 * wc + ip;
          bst<SC1>(rs, l16, OFF_PQ + ((tt * 32 + kb) * 2 + kh) * 1024, o);
        }
      }
    }
    if (wr == 1 && q < 2) st1(rs, lane * 4, OFF_RS + (p * 2 + (dq >> 1)) * 1024 + ((2 * (dq & 1) + wc) * 2 + kh) * 128, asu((dq >> 1) ? -rsum[0] : rsum[0]));
  }
  lds_add(C_RING);
  WAIT_V0();
  if (lane == 0) flag_set(ws, FLINE(LN_F1 + p) + (c * 4 + w) * 4, tag);
  if (idx < 2) code_warm(idx * 4 + w, lane, L_DUMMY);
  SCHED();
  if (w < 2) {
    if (jb < 176) {
      const int nkb = is1 ? NKB_X : NKB_H;
      const unsigned dst = (is1 ? OFF_W1 : OFF_W2) + ((jn0 >> 4) * nkb + (jk0 >> 5)) * 1024;
#pragma unroll
      for (int f = 0; f < 8; ++f) {
        const int ntl = f >> 1, kbl = f & 1;
        if (jk0 + kbl * 32 < (is1 ? D : F) && jn0 + (ntl >> 1) * 32 < (is1 ? F : D)) bst<SC1>(rs, l16, dst + (ntl * nkb + kbl) * 1024, R[f]);
      }
    }
    WAIT_V0();
    if (lds_inc(C_LATE) == 1u) { if (lane < 8) flag_set(ws, FLINE(LN_G3 + lane * 8 + (b >> 5)) + (b & 31) * 4, tag); }
  }
  wait_lds<true>(C_G3GO, 4);
  wait_words<16>(rs, gvo, FLINE(LN_G3 + xcd * 8 + w * 2), tag);
  lds_add(C_W1);
  const int ft0 = (c < 6 ? 11 * c : 66 + 10 * (c - 6)) + 3 * w;
  const int nft = w < 3 ? 3 : (c < 6 ? 2 : 1);
  constexpr int WK = 12;
  v4u w1f[WK][3];
  f32x4 accU[3];
  unsigned a1[3];
#pragma unroll
  for (int i = 0; i < 3; ++i) a1[i] = i < nft ? OFF_W1 + (ft0 + i) * NKB_X * 1024 : OOB;
  auto upd = [&](int k, v4u (&d)[3]) {
    const int kb = (k + 2 * c) & 15;
    const int ad = m == 8 ? L_MB + (512 + kb * 32 + q * 8) * 2 : (m == (kb >> 1) ? L_MB + (1024 + kb * 32 + q * 8) * 2 : L_ZERO);
    const v4u cf = *(const v4u*)(shm + ad);
#pragma unroll
    for (int i = 0; i < 3; ++i) accU[i] = mfma(cf, d[i], k == 0 ? f32x4{0, 0, 0, 0} : accU[i]);
  };
  wait_lds<true>(C_W1, 8);
#pragma unroll
  for (int k = 0; k < WK; ++k)
#pragma unroll
    for (int i = 0; i < 3; ++i) w1f[k][i] = bld<SC1>(rs, l16, a1[i] + ((k + 2 * c) & 15) * 1024);
  SCHED();
#pragma unroll
  for (int k = 0; k < WK; ++k) upd(k, w1f[k]);
  SCHED();
  wait_lds<true>(C_F2, 4);
#pragma unroll
  for (int i = 0; i < 3; ++i) {
    if (i < nft) {
#pragma unroll
      for (int k = WK; k < 16; ++k) glds16<SC1>(ws + a1[i] + ((k + 2 * c) & 15) * 1024 + l16, (LAS char*)shm + L_W1X + w * 12288 + ((k - WK) * 3 + i) * 1024);
    }
  }
  WAIT_V0();
  const char* w1x = shm + L_W1X + w * 12288 + l16;
#pragma unroll
  for (int k = WK; k < 16; ++k) {
    v4u t[3];
#pragma unroll
    for (int i = 0; i < 3; ++i) t[i] = *(const v4u*)(w1x + ((k - WK) * 3 + i) * 1024);
    upd(k, t);
  }
  v4u uf[3];
#pragma unroll
  for (int i = 0; i < 3; ++i) {
    const unsigned h0 = pk(accU[i][0], accU[i][1]), h1 = pk(accU[i][2], accU[i][3]);
    uf[i] = v4u{h0, h1, pk(accU[i][0] - bflo(h0), accU[i][1] - bfhi(h0)), pk(accU[i][2] - bflo(h1), accU[i][3] - bfhi(h1))};
  }
  SCHED();
  f32x4 acc[3][4];
  v4u hb[2][4];
  auto rdh = [&](int k) {
#pragma unroll
    for (int j = 0; j < 4; ++j) hb[k & 1][j] = *(const v4u*)(shm + L_H2 + j * H2W + k * 1024 + l16);
  };
  BAR();
  SCHED();
  rdh(0);
  static_for<0, 16>([&](auto ic) __attribute__((always_inline)) {
    constexpr int k = decltype(ic)::v;
    if (k == 3) BAR();
    SCHED();
    rdh(k + 1);
    if (k >= 1 && k <= 16 - WK) {
#pragma unroll
      for (int i = 0; i < 3; ++i) w1f[(k + WK - 1) % WK][i] = *(const v4u*)(w1x + ((k - 1) * 3 + i) * 1024);
    }
    SCHED();
#pragma unroll
    for (int j = 0; j < 4; ++j)
#pragma unroll
      for (int i = 0; i < 3; ++i) acc[i][j] = mfma(w1f[k % WK][i], hb[k & 1][j], k == 0 ? f32x4{0, 0, 0, 0} : acc[i][j]);
    SCHED();
  });
  float rsd[4];
#pragma unroll
  for (int j = 0; j < 4; ++j) rsd[j] = ((const float*)(shm + L_RSTD))[j * 16 + m];
#pragma unroll
  for (int i = 0; i < 3; ++i)
#pragma unroll
    for (int j = 0; j < 4; ++j) acc[i][j] = mfma(uf[i], hb[0][j], acc[i][j]);
  const unsigned vo = (q & 1) * (NKB_H * 1024) + ((q >> 1) * 16 + m) * 16;
#pragma unroll
  for (int i = 0; i < 3; ++i) {
    if (i < nft) {
      const int ft = ft0 + i;
#pragma unroll
      for (int j = 0; j < 4; j += 2) {
        const f32x4 a = acc[i][j] * rsd[j], bb = acc[i][j + 1] * rsd[j + 1];
        unsigned a0 = pk(fmaxf(a[0], 0.f), fmaxf(a[1], 0.f)), a1v = pk(fmaxf(a[2], 0.f), fmaxf(a[3], 0.f));
        unsigned b0 = pk(fmaxf(bb[0], 0.f), fmaxf(bb[1], 0.f)), b1 = pk(fmaxf(bb[2], 0.f), fmaxf(bb[3], 0.f));
        asm volatile("s_nop 1\n\tv_permlane16_swap_b32 %0, %2\n\tv_permlane16_swap_b32 %1, %3" : "+v"(a0), "+v"(a1v), "+v"(b0), "+v"(b1));
        bst<SC1>(rs, vo, OFF_H1 + ((4 * p + j) * NKB_H + (ft >> 1)) * 1024 + (ft & 1) * 512, v4u{a0, a1v, b0, b1});
      }
    }
  }
  WAIT_V0();
  if (lane == 0) flag_set(ws, FLINE(LN_F3 + p * 4 + (c >> 1)) + ((c & 1) * 4 + w) * 4, tag);
  lds_add(C_P4);
}

extern "C" void kernel_launch(void* const* d_in, const int* in_sizes, int n_in, void* d_out, int out_size, void* d_ws, size_t ws_size,
                              hipStream_t stream) {
  (void)hipFuncSetAttribute((const void*)fused_all, hipFuncAttributeMaxDynamicSharedMemorySize, K_LDS);
  fused_all<<<256, 512, K_LDS, stream>>>((const char*)d_in[0], (const char*)d_in[1], (const char*)d_in[2], (const char*)d_in[3], (const char*)d_in[4],
                                         (const char*)d_in[6], (const char*)d_in[7], (const char*)d_in[8], (const char*)d_in[9], (char*)d_ws, (char*)d_out);
}
```

```cpp
#include <hip/hip_runtime.h>
#include <hip/hip_bf16.h>
#include <stdint.h>

typedef unsigned v4u __attribute__((ext_vector_type(4)));
typedef unsigned v2u __attribute__((ext_vector_type(2)));
typedef float f32x4 __attribute__((ext_vector_type(4)));
typedef float f32x2 __attribute__((ext_vector_type(2)));
typedef short bf16x8 __attribute__((ext_vector_type(8)));
typedef __bf16 bf2 __attribute__((ext_vector_type(2)));
#define GAS __attribute__((address_space(1)))
#define LAS __attribute__((address_space(3)))
#define WAIT_V(n) asm volatile("s_waitcnt vmcnt(%0)" ::"n"(n) : "memory")
#define CFENCE() asm volatile("" ::: "memory")
#define WAIT_L0() do { CFENCE(); __builtin_amdgcn_s_waitcnt(0xC07F); } while (0)
#define WAIT_V0() do { WAIT_V(0); __builtin_amdgcn_s_waitcnt(0x0F70); CFENCE(); } while (0)
#define SCHED() __builtin_amdgcn_sched_barrier(0)
#define BAR() do { CFENCE(); __builtin_amdgcn_s_barrier(); CFENCE(); } while (0)
#define RSRC(p, n) __builtin_amdgcn_make_buffer_rsrc((void*)(p), 0, (n), 0x00020000)
constexpr int SC1 = 16, NT = 2;

constexpr int D = 512, F = 1376;
constexpr int NKB_X = 16, NKB_H = 43;
constexpr unsigned OFF_EPOCH = 0;
constexpr unsigned OFF_MOD  = 49152;
constexpr unsigned OFF_RS   = 131072;
constexpr unsigned OFF_ST   = 196608;
constexpr unsigned OFF_XNT  = 524288;
constexpr unsigned OFF_WD   = OFF_XNT + (2u << 20);
constexpr unsigned OFF_W1   = OFF_WD + (1u << 20);
constexpr unsigned OFF_W2   = OFF_W1 + 86 * 16 * 1024;
constexpr unsigned OFF_PQ   = OFF_W2 + 32 * 43 * 1024;
constexpr unsigned OFF_X1   = OFF_PQ + (8u << 20);
constexpr unsigned OFF_H1   = OFF_X1 + (2u << 20);
constexpr unsigned OFF_FL   = 24u << 20, FSTR = 256;
constexpr unsigned LN_G1 = 0, LN_G2 = 64, LN_G3 = 128;
constexpr unsigned LN_F1 = 192;
constexpr unsigned LN_F2 = 224, LN_F3 = 352;
constexpr unsigned LN_END = 480;
#define FLINE(l) (OFF_FL + (l) * FSTR)
constexpr unsigned WS_BYTES = FLINE(LN_END);
constexpr unsigned OOB = 0x4000000u;
constexpr int NS = 4, STAGE = 32768;
constexpr int L_TOP = NS * STAGE;
constexpr int L_RAW = L_TOP;
constexpr int L_MB = L_RAW + 14336;
constexpr int L_SL = L_MB + 3072;
constexpr int L_ZERO = L_TOP + 24576;
constexpr int L_CNT = L_ZERO + 64;
constexpr int L_RSTD = L_CNT + 64;
constexpr int L_DUMMY = L_RSTD + 256;
constexpr int K_LDS = L_DUMMY + 256;
constexpr int L_LN = 3 * STAGE;
constexpr int L_H2 = 0, H2W = 17 * 1024;
constexpr int L_W1X = 4 * H2W;
enum { C_G3GO = 0, C_G1 = 1, C_P4 = 2, C_JOB = 3, C_F1 = 4, C_MOD = 5, C_RING = 6, C_W1 = 7, C_F2 = 8, C_RED3 = 10, C_RED5 = 11, C_VEC = 13, C_LATE = 14 };
constexpr unsigned SPIN_CAP = 1u << 18;
constexpr int CODE_KB = 35;

template <int I> struct IC { static constexpr int v = I; };
template <int B, int E, class Fn> __device__ __forceinline__ void static_for(Fn&& f) { if constexpr (B < E) { f(IC<B>{}); static_for<B + 1, E>(f); } }
__device__ __forceinline__ float asf(unsigned u) { return __builtin_bit_cast(float, u); }
__device__ __forceinline__ unsigned asu(float f) { return __builtin_bit_cast(unsigned, f); }
__device__ __forceinline__ float bflo(unsigned u) { return asf(u << 16); }
__device__ __forceinline__ float bfhi(unsigned u) { return asf(u & 0xffff0000u); }
__device__ __forceinline__ unsigned pk(float lo, float hi) {
  f32x2 v = {lo, hi};
  return __builtin_bit_cast(unsigned, __builtin_convertvector(v, bf2));
}
__device__ __forceinline__ float rbf(float x) { return bflo(pk(x, x)); }
__device__ __forceinline__ f32x4 mfma(v4u a, v4u b, f32x4 c) {
  return __builtin_amdgcn_mfma_f32_16x16x32_bf16(__builtin_bit_cast(bf16x8, a), __builtin_bit_cast(bf16x8, b), c, 0, 0, 0);
}
template <int CTRL> __device__ __forceinline__ float dpp_add(float x) {
  int y = __builtin_amdgcn_update_dpp(0, __builtin_bit_cast(int, x), CTRL, 0xf, 0xf, false);
  return x + __builtin_bit_cast(float, y);
}
__device__ __forceinline__ float wave_sum(float x) {
  x = dpp_add<0x111>(x); x = dpp_add<0x112>(x); x = dpp_add<0x114>(x); x = dpp_add<0x118>(x);
  x = dpp_add<0x142>(x); x = dpp_add<0x143>(x);
  return __builtin_bit_cast(float, __builtin_amdgcn_readlane(__builtin_bit_cast(int, x), 63));
}
__device__ __forceinline__ float half_sum(float x) {
  float y = x;
  asm volatile("s_nop 1\n\tv_permlane32_swap_b32 %0, %1" : "+v"(x), "+v"(y));
  return x + y;
}
__device__ __forceinline__ float quad16_sum(float x) {
  float y = x;
  asm volatile("s_nop 1\n\tv_permlane16_swap_b32 %0, %1" : "+v"(x), "+v"(y));
  return half_sum(x + y);
}
__device__ __forceinline__ int fresh_lane() { int l; asm volatile("v_mbcnt_lo_u32_b32 %0, -1, 0\n\tv_mbcnt_hi_u32_b32 %0, -1, %0" : "=v"(l)); return l; }
template <int AUX> __device__ __forceinline__ v4u bld(__amdgpu_buffer_rsrc_t rs, unsigned voff, unsigned soff) { return __builtin_amdgcn_raw_buffer_load_b128(rs, voff, soff, AUX); }
template <int AUX> __device__ __forceinline__ void bst(__amdgpu_buffer_rsrc_t rs, unsigned voff, unsigned soff, v4u v) { __builtin_amdgcn_raw_buffer_store_b128(v, rs, voff + soff, 0, AUX); }
__device__ __forceinline__ void st1(__amdgpu_buffer_rsrc_t rs, unsigned voff, unsigned soff, unsigned v) { __builtin_amdgcn_raw_buffer_store_b32(v, rs, voff, soff, SC1); }
__device__ __forceinline__ unsigned ld1(__amdgpu_buffer_rsrc_t rs, unsigned voff, unsigned soff) { return __builtin_amdgcn_raw_buffer_load_b32(rs, voff, soff, SC1); }
template <int AUX> __device__ __forceinline__ void glds16(const char* g, LAS char* l) {
  __builtin_amdgcn_global_load_lds((const GAS unsigned*)g, (LAS unsigned*)l, 16, 0, AUX);
}
__device__ __forceinline__ void flag_set(char* ws, unsigned off, unsigned v) {
  asm volatile("global_atomic_swap %0, %1, %2 sc1" :: "v"(off), "v"(v), "s"(ws) : "memory");
}
template <int SLP> __device__ __forceinline__ void wait_words(__amdgpu_buffer_rsrc_t rs, unsigned voff, unsigned soff, unsigned tag) {
#pragma unroll 1
  for (unsigned spins = 0; spins < SPIN_CAP; ++spins) {
    CFENCE();
    const unsigned v = ld1(rs, voff, soff);
    if (__builtin_amdgcn_ballot_w64(v != tag) == 0ull) break;
    __builtin_amdgcn_s_sleep(SLP);
  }
  __builtin_amdgcn_fence(__ATOMIC_ACQUIRE, "wavefront");
  CFENCE();
  SCHED();
}
template <bool SLEEP> __device__ __forceinline__ void wait_lds(int idx, unsigned n) {
#pragma unroll 1
  for (unsigned spins = 0; spins < SPIN_CAP * 64; ++spins) {
    unsigned v;
    asm volatile("ds_read_b32 %0, %1\n\ts_waitcnt lgkmcnt(0)" : "=v"(v) : "v"(L_CNT + idx * 4) : "memory");
    if (__builtin_amdgcn_readfirstlane(v) >= n) break;
    if (SLEEP) __builtin_amdgcn_s_sleep(1);
  }
  SCHED();
}
__device__ __forceinline__ void lds_add(int idx) {
  unsigned long long ex;
  asm volatile("s_mov_b64 %0, exec\n\ts_mov_b64 exec, 1\n\tds_add_u32 %1, %2\n\ts_mov_b64 exec, %0" : "=&s"(ex) : "v"(L_CNT + idx * 4), "v"(1u) : "memory");
}
__device__ __forceinline__ unsigned lds_inc(int idx) {
  unsigned long long ex; unsigned old;
  asm volatile("s_mov_b64 %0, exec\n\ts_mov_b64 exec, 1\n\tds_add_rtn_u32 %1, %2, %3\n\ts_waitcnt lgkmcnt(0)\n\ts_mov_b64 exec, %0"
               : "=&s"(ex), "=&v"(old) : "v"(L_CNT + idx * 4), "v"(1u) : "memory");
  return __builtin_amdgcn_readfirstlane(old);
}

__global__ __launch_bounds__(512, 2) void fused_all(const char* __restrict__ x, const char* __restrict__ wsr, const char* __restrict__ wsi,
                                                    const char* __restrict__ wdr, const char* __restrict__ wdi,
                                                    const char* __restrict__ w1, const char* __restrict__ w2, const char* __restrict__ cvec,
                                                    const char* __restrict__ wmod, char* __restrict__ ws, char* __restrict__ out) {
  extern __shared__ __attribute__((aligned(16))) char shm[];
  const int b = blockIdx.x, xcd = b & 7, idx = b >> 3, pl = idx >> 3, p = xcd * 4 + pl, c = idx & 7, dq = c & 3, kh = c >> 2;
  const int w8 = __builtin_amdgcn_readfirstlane(threadIdx.x >> 6), w = w8 & 3;
  const __amdgpu_buffer_rsrc_t rs = RSRC(ws, WS_BYTES);
  const float* sll = (const float*)(shm + L_SL);

  unsigned long long pc0;
  asm volatile("s_getpc_b64 %0" : "=s"(pc0));
  auto code_warm = [&](int piece, int lane, int ldsoff) {
    const unsigned sl = piece * 64 + lane;
    if (sl < CODE_KB * 8) __builtin_amdgcn_global_load_lds((const GAS unsigned*)((pc0 & ~127ull) + (unsigned long long)sl * 128), (LAS unsigned*)((LAS char*)shm + ldsoff), 4, 0, 0);
  };
  if (idx == 0 && w8 < 5) code_warm(w8, threadIdx.x & 63, L_LN + 8192 + w8 * 256);
  SCHED();
  v4u xq; unsigned tq;
  asm volatile("global_load_dwordx4 %0, %2, %3\n\tglobal_load_dword %1, %4, %5 sc1"
               : "=&v"(xq), "=&v"(tq) : "v"(b * 8192 + threadIdx.x * 16), "s"(x), "v"(OFF_EPOCH), "s"(ws) : "memory");
  SCHED();
  v4u R[8], cvv;
  const int jb = c * 32 + pl * 8 + xcd;
  const int jm = ((c * 2 + (pl >> 1)) * 8 + xcd) * 2 + (pl & 1);
  const bool is1 = w == 0;
  const int jk0 = (is1 ? (jb & 7) : (jb >> 3)) * 64, jn0 = (is1 ? (jb >> 3) : (jb & 7)) * 64;
  {
    const int lane = threadIdx.x & 63, m = lane & 15, q = lane >> 4, kg = lane >> 3, ng = lane & 7;
    const char* base; unsigned off, ld, len;
    if (w8 >= 4 || (w >= 2 ? jm >= 192 : jb >= 240)) { base = cvec; ld = 0; off = 0; len = 0; }
    else if (w >= 2) { base = wmod; ld = 6144; len = 512 * 6144; off = ((jm / 96) * 256 + (w & 1) * 128 + (lane >> 2) * 8) * 6144 + (jm % 96) * 64 + (lane & 3) * 16; }
    else if (jb < 176) {
      base = is1 ? w1 : w2; ld = is1 ? F * 2 : D * 2; len = D * F * 2;
      const bool ok = (jk0 + kg * 8 < (is1 ? D : F)) && (jn0 + ng * 8 < (is1 ? F : D));
      off = ok ? (jk0 + kg * 8) * ld + (jn0 + ng * 8) * 2 : OOB;
    } else {
      const int j3 = w * 64 + jb - 176, et = j3 >> 2, kb0 = (j3 & 3) * 8;
      base = kb0 < 16 ? wdr : wdi; ld = 64; len = D * D * 2;
      off = (32 * (et >> 1) + 8 * (m >> 2) + 4 * (et & 1) + (m & 3)) * 1024 + (kb0 & 15) * 64 + q * 16;
    }
    ld = __builtin_amdgcn_readfirstlane(ld);
    len = __builtin_amdgcn_readfirstlane(len);
    const __amdgpu_buffer_rsrc_t rj = RSRC(base, len);
#pragma unroll
    for (int k = 0; k < 8; ++k) R[k] = bld<NT>(rj, off, k * ld);
    cvv = bld<0>(RSRC(cvec, 1024), ((w & 1) * 128 + (lane >> 2) * 8) * 2, (jm / 96 & 1) * 512);
    SCHED();
  }
  asm volatile("s_waitcnt vmcnt(9)" : "+v"(xq), "+v"(tq) :: "memory");
  unsigned tag;
  {
    const int tid = threadIdx.x, lane = tid & 63;
    float v[8];
#pragma unroll
    for (int u = 0; u < 4; ++u) { v[2 * u] = bflo(xq[u]); v[2 * u + 1] = bfhi(xq[u]); }
    const float mean = wave_sum(((v[0] + v[1]) + (v[2] + v[3])) + ((v[4] + v[5]) + (v[6] + v[7]))) * (1.f / D);
    float ss = 0.f;
#pragma unroll
    for (int j = 0; j < 8; ++j) { const float t = v[j] - mean; ss += t * t; }
    const float var = wave_sum(ss) * (1.f / D);
    const float mb = rbf(mean);
    const float rsd = 1.f / rbf(__builtin_sqrtf(rbf(rbf(var) + 1e-5f)));
    v4u o;
#pragma unroll
    for (int u = 0; u < 4; ++u) {
      const unsigned d2 = pk(v[2 * u] - mb, v[2 * u + 1] - mb);
      o[u] = pk(bflo(d2) * rsd, bfhi(d2) * rsd);
    }
    *(v4u*)(shm + L_LN + tid * 16) = o;
    if (tid < 32) ((unsigned*)(shm + L_ZERO))[tid] = 0u;
    WAIT_L0();
    BAR();
    const int d = (tid & ~31) + 8 * ((tid >> 2) & 3) + 4 * ((tid >> 4) & 1) + (tid & 3);
    const unsigned short* sp = (const unsigned short*)(shm + L_LN) + d;
    v4u t;
#pragma unroll
    for (int u = 0; u < 4; ++u) t[u] = (unsigned)sp[(2 * u) * 512] | ((unsigned)sp[(2 * u + 1) * 512] << 16);
    tag = __builtin_amdgcn_readfirstlane(tq) * 0x9E3779B1u + 0x7F4A7C15u;
    bst<SC1>(rs, tid * 16, OFF_XNT + b * 8192, t);
    WAIT_V0();
    BAR();
    if (w8 == 0) { if (lane < 8) flag_set(ws, FLINE(LN_G1 + lane * 8 + (b >> 5)) + (b & 31) * 4, tag); }
    else __builtin_amdgcn_s_sleep(1);
  }

  if (w8 >= 4) {
    const int lane = fresh_lane(), m = lane & 15, q = lane >> 4;
    const unsigned l16 = lane * 16;
    v4u wd[8][4], res[2];
    {
      const int gq = (w + dq + 1) & 3;
      const char* wbase = (gq < 2 ? wsr : wsi) + (size_t)(p * 64 + (gq & 1) * 32 + (lane >> 3)) * 4096 + kh * 2048;
      const int sl = lane & 7, h = lane >> 4;
      const char* wsrc0 = wbase + ((sl ^ h) * 16);
      const char* wsrc1 = wbase + ((sl ^ (4 + h)) * 16) + 8 * 4096;
      const char* xsrc = ws + OFF_XNT + ((size_t)(kh * 128 + 2 * w) * 512 + dq * 128 + lane) * 16;
      auto issW = [&](int st) {
        LAS char* dst = (LAS char*)shm + (st & 3) * STAGE + w * 4096 + 16384;
#pragma unroll
        for (int i = 0; i < 4; ++i) glds16<NT>(((i & 1) ? wsrc1 : wsrc0) + (size_t)(i >> 1) * 16 * 4096 + st * 128, dst + i * 1024);
      };
      auto issX = [&](int st) {
        LAS char* dst = (LAS char*)shm + (st & 3) * STAGE + w * 4096;
#pragma unroll
        for (int i = 0; i < 4; ++i) glds16<SC1>(xsrc + (size_t)st * 65536 + (i >> 1) * 8192 + (i & 1) * 1024, dst + i * 1024);
      };
      issW(0); issW(1); issW(2); issW(3);
#pragma unroll
      for (int ip = 0; ip < 2; ++ip) res[ip] = bld<0>(RSRC(x, 2048 * 1024), m * 1024 + q * 16 + ip * 64, (p * 64 + w * 16) * 1024 + c * 128);
      SCHED();
      if (w < 2) { wait_words<2>(rs, (lane >> 5) * FSTR + (lane & 31) * 4, FLINE(LN_G1 + xcd * 8 + kh * 4 + w * 2), tag); lds_add(C_G1); }
      wait_lds<true>(C_G1, 2);
      issX(0); issX(1); issX(2); issX(3);
      WAIT_V(12); BAR();
      WAIT_V(8); BAR();
      issX(4);
      WAIT_V(8); BAR();
      issW(4); issW(5);
      WAIT_V(12); BAR();
      issX(5); issX(6);
#pragma unroll 1
      for (int t = 3; t < 11; t += 2) {
        WAIT_V(12); BAR();
        issW(t + 3); issW(t + 4);
        WAIT_V(12); BAR();
        issX(t + 4); issX(t + 5);
      }
      WAIT_V(12); BAR();
      issW(14); issW(15);
      const unsigned a0 = OFF_WD + ((4 * c) * 32 + 8 * w) * 1024;
      auto ldwd = [&](int f0, int f1) {
        SCHED();
#pragma unroll
        for (int f = f0; f < f1; ++f) wd[f >> 2][f & 3] = bld<SC1>(rs, l16, a0 + ((f & 3) * 32 + (f >> 2)) * 1024);
        SCHED();
      };
      WAIT_V(12); BAR();
      issX(15);
      {
        const char* mp = ws + OFF_MOD + (w & 1) * 12288;
        LAS char* dst = (LAS char*)shm + L_RAW + (w & 1) * 7168;
        const unsigned sl_off = lane < 32 ? 2048 + dq * 512 + l16 : (lane < 48 ? 4096 + c * 256 + l16 - 512 : 10240 + c * 256 + l16 - 768);
#pragma unroll
        for (int i = 0; i < 4; ++i) {
          const int pc = (w >> 1) * 4 + i;
          const unsigned so = pc < 2 ? pc * 1024 + l16 : (pc < 6 ? 4096 + pc * 1024 + l16 : sl_off);
          glds16<SC1>(mp + so, dst + (pc < 6 ? pc : 6) * 1024);
        }
      }
      WAIT_V(12); BAR();
      ldwd(0, 12);
      WAIT_V(12); BAR();
      ldwd(12, 24);
      {
        const int tid = w * 64 + lane;
        f32x2 pa[3], pb[3];
        float sa1, sb1;
        asm volatile("ds_read_b64 %0, %8\n\tds_read_b64 %1, %8 offset:2048\n\tds_read_b64 %2, %8 offset:4096\n\t"
                     "ds_read_b64 %3, %8 offset:7168\n\tds_read_b64 %4, %8 offset:9216\n\tds_read_b64 %5, %8 offset:11264\n\t"
                     "ds_read_b32 %6, %9\n\tds_read_b32 %7, %9 offset:7168\n\ts_waitcnt lgkmcnt(0)"
                     : "=&v"(pa[0]), "=&v"(pa[1]), "=&v"(pa[2]), "=&v"(pb[0]), "=&v"(pb[1]), "=&v"(pb[2]), "=&v"(sa1), "=&v"(sb1)
                     : "v"(L_RAW + tid * 8), "v"(tid < 128 ? L_RAW + 6656 + tid * 4 : L_RAW + 4096 + c * 256 + (tid & 63) * 4) : "memory");
        unsigned z[3];
#pragma unroll
        for (int i = 0; i < 3; ++i) {
          z[i] = pk(pa[i][0] + pb[i][0], pa[i][1] + pb[i][1]);
          if (i == 2) z[i] = pk(bflo(z[i]) + 1.f, bfhi(z[i]) + 1.f);
        }
        float sv = rbf(sa1 + sb1);
        if (tid >= 128) sv = rbf(sv + 1.f);
        asm volatile("ds_write_b32 %0, %1\n\tds_write_b32 %0, %2 offset:1024\n\tds_write_b32 %0, %3 offset:2048\n\tds_write_b32 %0, %4 offset:3072"
                     :: "v"(L_MB + tid * 4), "v"(z[0]), "v"(z[1]), "v"(z[2]), "v"(sv) : "memory");
        lds_add(C_VEC);
      }
      wait_lds<true>(C_RING, 4);
      ldwd(24, 32);
      wait_lds<true>(C_VEC, 4);
    }
    SCHED();
    f32x4 acc[4], oth[4];
    f32x4* red = (f32x4*)(shm + L_H2);
    auto put = [&](int j, int ln) {
#pragma unroll
      for (int i = 0; i < 4; ++i) red[((j + w) & 3) * (H2W / 16) + ((j - 1) * 4 + i) * 64 + ln] = acc[i];
    };
    auto get = [&](int ln) {
#pragma unroll
      for (int i = 0; i < 4; ++i)
        oth[i] = (red[w * (H2W / 16) + i * 64 + ln] + red[w * (H2W / 16) + (4 + i) * 64 + ln]) + red[w * (H2W / 16) + (8 + i) * 64 + ln];
    };
    {
      f32x4 accS[4];
      v4u sf[8];
#pragma unroll
      for (int k = 0; k < 8; ++k) sf[k] = *(const v4u*)(shm + L_MB + ((8 * (w & 1) + k) * 32 + q * 8) * 2);
      SCHED();
#pragma unroll
      for (int k = 0; k < 8; ++k)
#pragma unroll
        for (int i = 0; i < 4; ++i) accS[i] = mfma(wd[k][i], sf[k], k == 0 ? f32x4{0, 0, 0, 0} : accS[i]);
      SCHED();
      if (w == 0) { wait_words<2>(rs, (lane & 31) * 4, FLINE(LN_F1 + p), tag); lds_add(C_F1); }
      else wait_lds<true>(C_F1, 1);
      constexpr int WIN = 12;
      f32x2 rr[4];
      v4u pq[WIN];
      unsigned b0[4];
#pragma unroll
      for (int j = 0; j < 4; ++j) b0[j] = OFF_PQ + ((4 * p + ((j + w) & 3)) * 64 + 16 * w) * 1024;
      auto ldr = [&](int j) {
#pragma unroll
        for (int h = 0; h < 2; ++h) rr[j][h] = asf(ld1(rs, m * 4 + h * 128, OFF_RS + (p * 2 + (w >> 1)) * 1024 + ((j + w) & 3) * 256));
      };
      auto ldp = [&](int n) { pq[n % WIN] = bld<SC1>(rs, l16, b0[3 - (n >> 4)] + (n & 15) * 1024); };
      ldr(3);
      SCHED();
#pragma unroll
      for (int n = 0; n < WIN; ++n) ldp(n);
      SCHED();
      ldr(2); ldr(1); ldr(0);
      SCHED();
      static_for<0, 64>([&](auto ic) __attribute__((always_inline)) {
        constexpr int n = decltype(ic)::v, j = 3 - (n >> 4), g = n & 15;
        const v4u cur = pq[n % WIN];
        if (g == 0 && j < 3) {
          f32x4 nx[4];
#pragma unroll
          for (int i = 0; i < 4; ++i) nx[i] = mfma(wd[0][i], cur, accS[i] * (rr[j][0] + rr[j][1]));
          put(j + 1, lane);
          if (j == 0) lds_add(C_RED3);
#pragma unroll
          for (int i = 0; i < 4; ++i) acc[i] = nx[i];
        } else {
#pragma unroll
          for (int i = 0; i < 4; ++i) acc[i] = mfma(wd[g >> 1][i], cur, n == 0 ? accS[i] * (rr[j][0] + rr[j][1]) : acc[i]);
        }
        SCHED();
        if (n + WIN < 64) ldp(n + WIN);
        if (n + WIN == 63) lds_add(C_W1);
        if (n == 40) lds_add(C_G3GO);
        if (n == 58) { asm volatile("" : "+v"(acc[0]), "+v"(acc[1]), "+v"(acc[2]), "+v"(acc[3])); wait_lds<false>(C_RED3, 4); get(lane); }
        SCHED();
      });
    }
    auto gate = [&](const f32x4 (&gv)[4]) {
#pragma unroll
      for (int i = 0; i < 4; ++i) {
        const f32x4 t = (acc[i] + oth[i]) * gv[i];
        const unsigned r0 = res[i >> 1][2 * (i & 1)], r1 = res[i >> 1][2 * (i & 1) + 1];
        res[i >> 1][2 * (i & 1)] = pk(bflo(r0) + t[0], bfhi(r0) + t[1]);
        res[i >> 1][2 * (i & 1) + 1] = pk(bflo(r1) + t[2], bfhi(r1) + t[3]);
      }
    };
    const unsigned xb = OFF_X1 + (4 * p + w) * NKB_X * 1024, sb0 = OFF_ST + (p * 4 + w) * 1024;
    {
      f32x4 cv[4], gv[4];
#pragma unroll
      for (int i = 0; i < 4; ++i) { gv[i] = *(const f32x4*)(sll + 32 * (i >> 1) + 8 * q + 4 * (i & 1)); cv[i] = *(const f32x4*)(sll + 128 + 32 * (i >> 1) + 8 * q + 4 * (i & 1)); }
      gate(gv);
      float v[16];
#pragma unroll
      for (int u = 0; u < 8; ++u) { v[2 * u] = bflo(res[u >> 2][u & 3]); v[2 * u + 1] = bfhi(res[u >> 2][u & 3]); }
      float s = 0.f;
#pragma unroll
      for (int u = 0; u < 16; u += 4) s += (v[u] + v[u + 1]) + (v[u + 2] + v[u + 3]);
      const float mc = quad16_sum(s) * (1.f / 64);
#pragma unroll
      for (int u = 0; u < 16; ++u) v[u] -= mc;
#pragma unroll
      for (int ip = 0; ip < 2; ++ip) {
        v4u o;
#pragma unroll
        for (int u = 0; u < 4; ++u) o[u] = pk(v[8 * ip + 2 * u] * cv[2 * ip + (u >> 1)][(2 * u) & 3], v[8 * ip + 2 * u + 1] * cv[2 * ip + (u >> 1)][(2 * u + 1) & 3]);
        bst<SC1>(rs, l16, xb + (2 * c + ip) * 1024, o);
        *(v4u*)(shm + L_H2 + w * H2W + ip * 1024 + l16) = o;
      }
      float ss = 0.f, s2 = 0.f;
#pragma unroll
      for (int u = 0; u < 16; u += 2) { ss += v[u] * v[u]; s2 += v[u + 1] * v[u + 1]; }
      ss = quad16_sum(ss + s2);
      if (q == 0) __builtin_amdgcn_raw_buffer_store_b64(v2u{asu(mc), asu(ss)}, rs, m * 8, sb0 + c * 128, SC1);
      WAIT_V0();
      if (lane == 0) flag_set(ws, FLINE(LN_F2 + p * 4 + w) + c * 4, tag);
      lds_add(C_F2);
    }
    asm volatile("" : "+v"(res[0]), "+v"(res[1]));
    SCHED();
    v4u w2f[11][4];
    const unsigned a0 = OFF_W2 + ((4 * c) * NKB_H + 11 * w) * 1024;
    const unsigned last = w == 3 ? OOB : 10 * 1024;
    auto ldw2 = [&](int k0, int k1) {
#pragma unroll
      for (int k = k0; k < k1; ++k)
#pragma unroll
        for (int i = 0; i < 4; ++i) w2f[k][i] = bld<SC1>(rs, l16, a0 + i * NKB_H * 1024 + (k < 10 ? k * 1024 : last));
      SCHED();
    };
    __builtin_amdgcn_s_sleep(10);
    wait_words<2>(rs, (lane & 7) * 4, FLINE(LN_F2 + p * 4 + w), tag);
    {
      v4u X[14];
      v2u st[8];
#pragma unroll
      for (int k = 0; k < 14; ++k) X[k] = bld<SC1>(rs, l16, xb + ((2 * c + 2 + k) & 15) * 1024);
#pragma unroll
      for (int i = 0; i < 8; ++i) st[i] = __builtin_amdgcn_raw_buffer_load_b64(rs, m * 8 + i * 128, sb0, SC1);
      SCHED();
#pragma unroll
      for (int k = 0; k < 14; ++k) {
        *(v4u*)(shm + L_H2 + w * H2W + (2 + k) * 1024 + l16) = X[k];
        if (k == 1) { WAIT_L0(); BAR(); SCHED(); ldw2(0, 2); }
      }
      WAIT_L0(); BAR(); SCHED();
      ldw2(2, 9);
      float mu[8], mean = 0.f, m2 = 0.f;
#pragma unroll
      for (int i = 0; i < 8; ++i) { mu[i] = asf(st[i].x); mean += mu[i]; m2 += asf(st[i].y); }
      mean *= 0.125f;
      float dv = 0.f;
#pragma unroll
      for (int i = 0; i < 8; ++i) { const float d0 = mu[i] - mean; dv += d0 * d0; }
      m2 += 64.f * dv;
      const float den = rbf(__builtin_sqrtf(rbf(rbf(m2 * (1.f / D)) + 1e-5f)));
      const float mb = rbf(mean);
      unsigned d0 = q == 0 ? pk(mu[0] - mb, mu[1] - mb) : pk(mu[4] - mb, mu[5] - mb);
      unsigned d1 = q == 0 ? pk(mu[2] - mb, mu[3] - mb) : pk(mu[6] - mb, mu[7] - mb);
      if (q >= 2) { d0 = q == 2 ? pk(den, 0.f) : 0u; d1 = 0u; }
      *(v4u*)(shm + L_H2 + w * H2W + 16 * 1024 + l16) = v4u{d0, d1, d0, d1};
      if (q == 0) ((float*)(shm + L_RSTD))[w * 16 + m] = 1.f / den;
      WAIT_L0(); BAR(); SCHED();
    }
    {
      constexpr int WIN = 8;
      ldw2(9, 11);
      wait_lds<true>(C_P4, 4);
      const int ln = fresh_lane();
      __builtin_amdgcn_s_sleep(14);
      wait_words<2>(rs, (ln & 7) * 4, FLINE(LN_F3 + p * 4 + w), tag);
      v4u hf[WIN];
      unsigned b0[4];
#pragma unroll
      for (int j = 0; j < 4; ++j) b0[j] = OFF_H1 + ((4 * p + ((j + w) & 3)) * NKB_H + 11 * w) * 1024;
      auto ldh = [&](int n) { hf[n % WIN] = bld<SC1>(rs, ln * 16, b0[3 - n / 11] + (n % 11 < 10 ? (n % 11) * 1024 : last)); };
#pragma unroll
      for (int n = 0; n < WIN; ++n) ldh(n);
      SCHED();
      static_for<0, 44>([&](auto ic) __attribute__((always_inline)) {
        constexpr int n = decltype(ic)::v, j = 3 - n / 11, g = n % 11;
        const v4u cur = hf[n % WIN];
        if (g == 0 && j < 3) {
          f32x4 nx[4];
#pragma unroll
          for (int i = 0; i < 4; ++i) nx[i] = mfma(w2f[0][i], cur, f32x4{0, 0, 0, 0});
          put(j + 1, ln);
          if (j == 0) lds_add(C_RED5);
#pragma unroll
          for (int i = 0; i < 4; ++i) acc[i] = nx[i];
        } else {
#pragma unroll
          for (int i = 0; i < 4; ++i) acc[i] = mfma(w2f[g][i], cur, n == 0 ? f32x4{0, 0, 0, 0} : acc[i]);
        }
        SCHED();
        if (n + WIN < 44) ldh(n + WIN);
        if (n == 40) { asm volatile("" : "+v"(acc[0]), "+v"(acc[1]), "+v"(acc[2]), "+v"(acc[3])); wait_lds<false>(C_RED5, 4); get(ln); }
        SCHED();
      });
      f32x4 gv[4];
#pragma unroll
      for (int i = 0; i < 4; ++i) gv[i] = *(const f32x4*)(sll + 64 + 32 * (i >> 1) + 8 * (ln >> 4) + 4 * (i & 1));
      gate(gv);
#pragma unroll
      for (int ip = 0; ip < 2; ++ip) bst<0>(RSRC(out, 2048 * 1024), (ln & 15) * 1024 + (ln >> 4) * 16 + ip * 64, (p * 64 + w * 16) * 1024 + c * 128, res[ip]);
      if (b == 0 && w == 0 && ln == 0) st1(rs, 0, OFF_EPOCH, tag);
    }
    return;
  }

  const int lane = fresh_lane(), m = lane & 15, q = lane >> 4;
  const unsigned l16 = lane * 16;
  const int wr = w >> 1, wc = w & 1;
  if (w >= 2) {
    if (jm < 192) {
      float a8[8] = {0.f, 0.f, 0.f, 0.f, 0.f, 0.f, 0.f, 0.f};
#pragma unroll
      for (int i = 0; i < 8; ++i) {
        const float c0 = (i & 1) ? bfhi(cvv[i >> 1]) : bflo(cvv[i >> 1]);
        const float sl = rbf(c0 / (1.f + __expf(-c0)));
#pragma unroll
        for (int u = 0; u < 4; ++u) { a8[2 * u] += sl * bflo(R[i][u]); a8[2 * u + 1] += sl * bfhi(R[i][u]); }
      }
      float* red = (float*)(shm + L_TOP + 16384);
      *(f32x4*)(red + ((w & 1) * 16 + (lane >> 2)) * 36 + (lane & 3) * 8) = f32x4{a8[0], a8[1], a8[2], a8[3]};
      *(f32x4*)(red + ((w & 1) * 16 + (lane >> 2)) * 36 + (lane & 3) * 8 + 4) = f32x4{a8[4], a8[5], a8[6], a8[7]};
      if (lds_inc(C_MOD) == 1u) {
        float s = 0.f;
#pragma unroll
        for (int i = 0; i < 16; ++i) s += red[((lane >> 5) * 16 + i) * 36 + (lane & 31)];
        s = half_sum(s);
        if (lane < 32) st1(rs, lane * 4, OFF_MOD + ((jm / 96) * 3072 + (jm % 96) * 32) * 4, asu(s));
      }
    }
  } else if (jb < 176) {
    const int kg = lane >> 3, ng = lane & 7;
    char* lds = shm + L_TOP + w * 8192;
    const int a = ng & 3, qq = kg & 3;
    char* wp = lds + ((ng >> 2) * 4 + (kg >> 2)) * 1024 + (is1 ? (a >> 1) * 2048 + (qq * 16 + 8 * (a & 1)) * 16 : (qq * 16 + 4 * a) * 16);
    const int hi = is1 ? 64 : 2048;
#pragma unroll
    for (int jn = 0; jn < 8; ++jn) {
      const unsigned sel = (jn & 1) ? 0x07060302u : 0x05040100u;
      const int u = jn >> 1;
      v4u o;
      o.x = __builtin_amdgcn_perm(R[1][u], R[0][u], sel);
      o.y = __builtin_amdgcn_perm(R[3][u], R[2][u], sel);
      o.z = __builtin_amdgcn_perm(R[5][u], R[4][u], sel);
      o.w = __builtin_amdgcn_perm(R[7][u], R[6][u], sel);
      *(v4u*)(wp + (jn >> 2) * hi + (jn & 3) * 16) = o;
    }
    WAIT_L0();
    CFENCE();
#pragma unroll
    for (int f = 0; f < 8; ++f) R[f] = *(const v4u*)(lds + f * 1024 + l16);
  } else if (jb < 240) {
#pragma unroll
    for (int i = 0; i < 8; ++i) bst<SC1>(rs, l16, OFF_WD + ((w * 64 + jb - 176) * 8 + i) * 1024, R[i]);
  }
  SCHED();
  const unsigned gvo = (lane >> 5) * FSTR + (lane & 31) * 4;
  {
    const int xoff = q * 2048 + (wc * 64 + m) * 16;
    int wo[2];
#pragma unroll
    for (int kk = 0; kk < 2; ++kk) wo[kk] = 16384 + wr * 8192 + m * 128 + (((4 * kk + q) ^ ((m >> 1) & 7)) * 16);
    f32x4 acc[4][4], rsum = {0, 0, 0, 0};
#pragma unroll
    for (int i = 0; i < 4; ++i)
#pragma unroll
      for (int j = 0; j < 4; ++j) acc[i][j] = f32x4{0, 0, 0, 0};
    const v4u ones = {0x3f803f80u, 0x3f803f80u, 0x3f803f80u, 0x3f803f80u};
    v4u XA[4], WA[4], XB[4], WB[4];
    int sA = 0;
    auto rd = [&](int kk, v4u (&X)[4], v4u (&W)[4]) {
      W[0] = *(const v4u*)(shm + sA + wo[kk]);
      W[1] = *(const v4u*)(shm + sA + wo[kk] + 2048);
      W[2] = *(const v4u*)(shm + sA + wo[kk] + 6144 - wc * 2048);
      W[3] = *(const v4u*)(shm + sA + wo[kk] + 4096 + wc * 2048);
#pragma unroll
      for (int j = 0; j < 4; ++j) X[j] = *(const v4u*)(shm + sA + xoff + kk * 8192 + j * 256);
      if (kk) sA = (sA + STAGE) & (NS * STAGE - 1);
    };
    auto mm = [&](v4u (&X)[4], v4u (&W)[4]) {
#pragma unroll
      for (int j = 0; j < 4; ++j)
#pragma unroll
        for (int i = 0; i < 4; ++i) acc[i][j] = mfma(X[i], W[j], acc[i][j]);
      rsum = mfma(ones, W[3], rsum);
    };
    auto mix = [&]() {
#pragma unroll
      for (int i = 0; i < 8; ++i) {
        __builtin_amdgcn_sched_group_barrier(0x008, 1, 0);
        __builtin_amdgcn_sched_group_barrier(0x100, 1, 0);
      }
      __builtin_amdgcn_sched_group_barrier(0x008, 9, 0);
    };
    auto steps = [&](int n) {
#pragma unroll 1
      for (int t = 0; t < n; ++t) {
        SCHED();
        rd(1, XB, WB); mm(XA, WA); mix();
        SCHED();
        WAIT_L0();
        BAR();
        SCHED();
        rd(0, XA, WA); mm(XB, WB); mix();
      }
      SCHED();
    };
    BAR();
    rd(0, XA, WA);
    steps(2);
    WAIT_V0();
    if (lds_inc(C_JOB) == 3u) { if (lane < 8) flag_set(ws, FLINE(LN_G2 + lane * 8 + (b >> 5)) + (b & 31) * 4, tag); }
    steps(7);
    if (idx < 2) code_warm(idx * 4 + w, lane, L_DUMMY);
    const unsigned g2 = ld1(rs, gvo, FLINE(LN_G2 + xcd * 8 + w * 2));
    steps(2);
    if (__builtin_amdgcn_ballot_w64(g2 != tag) != 0ull) wait_words<2>(rs, gvo, FLINE(LN_G2 + xcd * 8 + w * 2), tag);
    CFENCE();
    steps(4);
    rd(1, XB, WB); mm(XA, WA); mix();
    SCHED();
    mm(XB, WB);
    f32x4 cav[4];
#pragma unroll
    for (int i = 0; i < 4; ++i) {
      const float* ml = (const float*)(shm + L_RAW + 6144) + wc * 64 + 32 * (i >> 1) + 8 * q + 4 * (i & 1);
      const f32x4 t = *(const f32x4*)ml + *(const f32x4*)(ml + 1792);
      const unsigned u0 = pk(t[0], t[1]), u1 = pk(t[2], t[3]);
      const unsigned z0 = pk(bflo(u0) + 1.f, bfhi(u0) + 1.f), z1 = pk(bflo(u1) + 1.f, bfhi(u1) + 1.f);
      cav[i] = f32x4{bflo(z0), bfhi(z0), bflo(z1), bfhi(z1)};
    }
#pragma unroll
    for (int jh = 0; jh < 2; ++jh) {
      const int gq = (2 * wr + jh + dq + 1) & 3;
      const float sg = (gq >> 1) ? -1.f : 1.f;
#pragma unroll
      for (int ip = 0; ip < 2; ++ip) {
        const f32x4 c0 = cav[2 * ip] * sg, c1 = cav[2 * ip + 1] * sg;
#pragma unroll
        for (int jl = 0; jl < 2; ++jl) {
          const int j = 2 * jh + jl;
          const int u = j < 2 ? j : (j == 3 ? wc : 1 - wc);
          const f32x4 a0 = acc[2 * ip][j] * c0, a1 = acc[2 * ip + 1][j] * c1;
          v4u o; o.x = pk(a0[0], a0[1]); o.y = pk(a0[2], a0[3]); o.z = pk(a1[0], a1[1]); o.w = pk(a1[2], a1[3]);
          const int tt = 4 * p + 2 * (gq & 1) + u, kb = (gq >> 1) * 16 + 4 * dq + 2 * wc + ip;
          bst<SC1>(rs, l16, OFF_PQ + ((tt * 32 + kb) * 2 + kh) * 1024, o);
        }
      }
    }
    if (wr == 1 && q < 2) st1(rs, lane * 4, OFF_RS + (p * 2 + (dq >> 1)) * 1024 + ((2 * (dq & 1) + wc) * 2 + kh) * 128, asu((dq >> 1) ? -rsum[0] : rsum[0]));
  }
  lds_add(C_RING);
  WAIT_V0();
  if (lane == 0) flag_set(ws, FLINE(LN_F1 + p) + (c * 4 + w) * 4, tag);
  if (idx < 2) code_warm(idx * 4 + w, lane, L_DUMMY);
  SCHED();
  if (w < 2) {
    if (jb < 176) {
      const int nkb = is1 ? NKB_X : NKB_H;
      const unsigned dst = (is1 ? OFF_W1 : OFF_W2) + ((jn0 >> 4) * nkb + (jk0 >> 5)) * 1024;
#pragma unroll
      for (int f = 0; f < 8; ++f) {
        const int ntl = f >> 1, kbl = f & 1;
        if (jk0 + kbl * 32 < (is1 ? D : F) && jn0 + (ntl >> 1) * 32 < (is1 ? F : D)) bst<SC1>(rs, l16, dst + (ntl * nkb + kbl) * 1024, R[f]);
      }
    }
    WAIT_V0();
    if (lds_inc(C_LATE) == 1u) { if (lane < 8) flag_set(ws, FLINE(LN_G3 + lane * 8 + (b >> 5)) + (b & 31) * 4, tag); }
  }
  wait_lds<true>(C_G3GO, 4);
  wait_words<16>(rs, gvo, FLINE(LN_G3 + xcd * 8 + w * 2), tag);
  lds_add(C_W1);
  const int ft0 = (c < 6 ? 11 * c : 66 + 10 * (c - 6)) + 3 * w;
  const int nft = w < 3 ? 3 : (c < 6 ? 2 : 1);
  constexpr int WK = 12;
  v4u w1f[WK][3];
  f32x4 accU[3];
  unsigned a1[3];
#pragma unroll
  for (int i = 0; i < 3; ++i) a1[i] = i < nft ? OFF_W1 + (ft0 + i) * NKB_X * 1024 : OOB;
  auto upd = [&](int k, v4u (&d)[3]) {
    const int kb = (k + 2 * c) & 15;
    const int ad = m == 8 ? L_MB + (512 + kb * 32 + q * 8) * 2 : (m == (kb >> 1) ? L_MB + (1024 + kb * 32 + q * 8) * 2 : L_ZERO);
    const v4u cf = *(const v4u*)(shm + ad);
#pragma unroll
    for (int i = 0; i < 3; ++i) accU[i] = mfma(cf, d[i], k == 0 ? f32x4{0, 0, 0, 0} : accU[i]);
  };
  wait_lds<true>(C_W1, 8);
#pragma unroll
  for (int k = 0; k < WK; ++k)
#pragma unroll
    for (int i = 0; i < 3; ++i) w1f[k][i] = bld<SC1>(rs, l16, a1[i] + ((k + 2 * c) & 15) * 1024);
  SCHED();
#pragma unroll
  for (int k = 0; k < WK; ++k) upd(k, w1f[k]);
  SCHED();
  wait_lds<true>(C_F2, 4);
#pragma unroll
  for (int i = 0; i < 3; ++i) {
    if (i < nft) {
#pragma unroll
      for (int k = WK; k < 16; ++k) glds16<SC1>(ws + a1[i] + ((k + 2 * c) & 15) * 1024 + l16, (LAS char*)shm + L_W1X + w * 12288 + ((k - WK) * 3 + i) * 1024);
    }
  }
  WAIT_V0();
  const char* w1x = shm + L_W1X + w * 12288 + l16;
#pragma unroll
  for (int k = WK; k < 16; ++k) {
    v4u t[3];
#pragma unroll
    for (int i = 0; i < 3; ++i) t[i] = *(const v4u*)(w1x + ((k - WK) * 3 + i) * 1024);
    upd(k, t);
  }
  v4u uf[3];
#pragma unroll
  for (int i = 0; i < 3; ++i) {
    const unsigned h0 = pk(accU[i][0], accU[i][1]), h1 = pk(accU[i][2], accU[i][3]);
    uf[i] = v4u{h0, h1, pk(accU[i][0] - bflo(h0), accU[i][1] - bfhi(h0)), pk(accU[i][2] - bflo(h1), accU[i][3] - bfhi(h1))};
  }
  SCHED();
  f32x4 acc[3][4];
  v4u hb[2][4];
  auto rdh = [&](int k) {
#pragma unroll
    for (int j = 0; j < 4; ++j) hb[k & 1][j] = *(const v4u*)(shm + L_H2 + j * H2W + k * 1024 + l16);
  };
  BAR();
  SCHED();
  rdh(0);
  static_for<0, 16>([&](auto ic) __attribute__((always_inline)) {
    constexpr int k = decltype(ic)::v;
    if (k == 3 || k == 14) BAR();
    SCHED();
    rdh(k + 1);
    if (k >= 1 && k <= 16 - WK) {
#pragma unroll
      for (int i = 0; i < 3; ++i) w1f[(k + WK - 1) % WK][i] = *(const v4u*)(w1x + ((k - 1) * 3 + i) * 1024);
    }
    SCHED();
#pragma unroll
    for (int j = 0; j < 4; ++j)
#pragma unroll
      for (int i = 0; i < 3; ++i) acc[i][j] = mfma(w1f[k % WK][i], hb[k & 1][j], k == 0 ? f32x4{0, 0, 0, 0} : acc[i][j]);
    SCHED();
  });
  float rsd[4];
#pragma unroll
  for (int j = 0; j < 4; ++j) rsd[j] = ((const float*)(shm + L_RSTD))[j * 16 + m];
#pragma unroll
  for (int i = 0; i < 3; ++i)
#pragma unroll
    for (int j = 0; j < 4; ++j) acc[i][j] = mfma(uf[i], hb[0][j], acc[i][j]);
  const unsigned vo = (q & 1) * (NKB_H * 1024) + ((q >> 1) * 16 + m) * 16;
#pragma unroll
  for (int i = 0; i < 3; ++i) {
    if (i < nft) {
      const int ft = ft0 + i;
#pragma unroll
      for (int j = 0; j < 4; j += 2) {
        const f32x4 a = acc[i][j] * rsd[j], bb = acc[i][j + 1] * rsd[j + 1];
        unsigned a0 = pk(fmaxf(a[0], 0.f), fmaxf(a[1], 0.f)), a1v = pk(fmaxf(a[2], 0.f), fmaxf(a[3], 0.f));
        unsigned b0 = pk(fmaxf(bb[0], 0.f), fmaxf(bb[1], 0.f)), b1 = pk(fmaxf(bb[2], 0.f), fmaxf(bb[3], 0.f));
        asm volatile("s_nop 1\n\tv_permlane16_swap_b32 %0, %2\n\tv_permlane16_swap_b32 %1, %3" : "+v"(a0), "+v"(a1v), "+v"(b0), "+v"(b1));
        bst<SC1>(rs, vo, OFF_H1 + ((4 * p + j) * NKB_H + (ft >> 1)) * 1024 + (ft & 1) * 512, v4u{a0, a1v, b0, b1});
      }
    }
  }
  WAIT_V0();
  if (lane == 0) flag_set(ws, FLINE(LN_F3 + p * 4 + (c >> 1)) + ((c & 1) * 4 + w) * 4, tag);
  lds_add(C_P4);
}

extern "C" void kernel_launch(void* const* d_in, const int* in_sizes, int n_in, void* d_out, int out_size, void* d_ws, size_t ws_size,
                              hipStream_t stream) {
  (void)hipFuncSetAttribute((const void*)fused_all, hipFuncAttributeMaxDynamicSharedMemorySize, K_LDS);
  fused_all<<<256, 512, K_LDS, stream>>>((const char*)d_in[0], (const char*)d_in[1], (const char*)d_in[2], (const char*)d_in[3], (const char*)d_in[4],
                                         (const char*)d_in[6], (const char*)d_in[7], (const char*)d_in[8], (const char*)d_in[9], (char*)d_ws, (char*)d_out);
}
```
